```python
import torch

def _softmax(x, axis=-1):
    return torch.softmax(x, dim=axis)
def _gelu(x):
    return torch.nn.functional.gelu(x, approximate="tanh")
def _silu(x):
    return torch.nn.functional.silu(x)
def _sigmoid(x):
    return torch.sigmoid(x)
def _layernorm(x, axis=-1, eps=1e-5):
    m = x.mean(dim=axis, keepdim=True)
    v = x.var(dim=axis, keepdim=True, unbiased=False)
    return (x - m) / torch.sqrt(v + eps)
def _scatter_add(tgt, idx, src, axis=0):
    return tgt.clone().index_add_(axis, idx, src)
def _segment_sum(src, seg, n):
    out = torch.zeros((n,) + src.shape[1:], dtype=src.dtype)
    return out.index_add_(0, seg, src)
def _pad(x, pw):
    flat = tuple(v for pair in reversed(pw) for v in pair)
    return torch.nn.functional.pad(x, flat)

kernel_name = 'recurrent_scan__mha_block__dense_activation_relu__prompt'

def setup_inputs():

    _w = (lambda a: (lambda t: t.to(torch.bfloat16) if t.is_floating_point() else t)(torch.as_tensor(a)))
    def _A_setup():
        B, S, D, H = 1, 2048, 640, 32
        return {
            "x": _w((lambda *s: torch.randn(*s, dtype=torch.bfloat16) * 0.05)(B, S, D)),
            "wqkv": _w((lambda *s: torch.randn(*s, dtype=torch.bfloat16) * 0.05)(D, 3 * D)),
            "wo": _w((lambda *s: torch.randn(*s, dtype=torch.bfloat16) * 0.05)(D, D)),
        }
    _A_in = _A_setup()

    def _B_setup():
        B, S, D, Dff = 1, 2048, 640, 1536
        return {
            "x": _w((lambda *s: torch.randn(*s, dtype=torch.bfloat16) * 0.05)(B, S, D)),
            "w1": _w((lambda *s: torch.randn(*s, dtype=torch.bfloat16) * 0.05)(D, Dff)),
            "w2": _w((lambda *s: torch.randn(*s, dtype=torch.bfloat16) * 0.05)(Dff, D)),
        }
    _B_in = _B_setup()

    return {
        "A_x": _A_in["x"],
        "A_wqkv": _A_in["wqkv"],
        "A_wo": _A_in["wo"],
        "B_x": _B_in["x"],
        "B_w1": _B_in["w1"],
        "B_w2": _B_in["w2"],
    }

def reference(A_x, A_wqkv, A_wo, B_x, B_w1, B_w2):
    def _A_ref(x, wqkv, wo):
        B, S, D = x.shape
        H = 32
        Dh = D // H
        qkv = (lambda x, *d: x.permute(*d))((x @ wqkv).reshape(B, S, 3, H, Dh), 2, 0, 3, 1, 4)
        q, k, v = qkv[0], qkv[1], qkv[2]
        scores = torch.einsum("bhqd,bhkd->bhqk", q, k) * (Dh ** -0.5)
        p = _softmax(scores, axis=-1)
        o = torch.einsum("bhqk,bhkd->bhqd", p, v)
        return (lambda x, *d: x.permute(*d))(o, 0, 2, 1, 3).reshape(B, S, D) @ wo

    def _B_ref(x, w1, w2):
        h = torch.einsum("bsd,df->bsf", x, w1)
        zero = torch.zeros_like(h)
        h = torch.maximum(h, zero)
        return torch.einsum("bsf,fd->bsd", h, w2)

    x = A_x
    xn = _layernorm(x, axis=-1)
    s = _A_ref(xn, A_wqkv, A_wo)
    g = _silu(_B_ref(xn, B_w1, B_w2))
    return x + s * g
```

```cpp
#include <hip/hip_runtime.h>
#include <hip/hip_bf16.h>
namespace ref {
constexpr int S = 2048, D = 640, H = 32, DH = 20, FF = 1536;
__device__ inline float b2f(unsigned short u) { return __uint_as_float((unsigned)u << 16); }
__global__ void ln(const unsigned short* x, float* xn) {
  int row = blockIdx.x; __shared__ float red[64];
  float v[10], s = 0; for (int i = 0; i < 10; ++i) { v[i] = b2f(x[row * D + threadIdx.x + 64 * i]); s += v[i]; }
  for (int o = 32; o; o >>= 1) s += __shfl_xor(s, o);
  float m = s / D, q = 0; for (int i = 0; i < 10; ++i) q += (v[i] - m) * (v[i] - m);
  for (int o = 32; o; o >>= 1) q += __shfl_xor(q, o);
  float r = 1.0f / sqrtf(q / D + 1e-5f);
  for (int i = 0; i < 10; ++i) xn[row * D + threadIdx.x + 64 * i] = (v[i] - m) * r;
}
__global__ void gemm(const float* A, const unsigned short* W, float* C, int K, int N, int relu) {
  __shared__ float a[16][65];
  int n = blockIdx.x * 256 + threadIdx.x, m0 = blockIdx.y * 16;
  float acc[16]; for (int i = 0; i < 16; ++i) acc[i] = 0;
  for (int k0 = 0; k0 < K; k0 += 64) {
    __syncthreads();
    for (int i = threadIdx.x; i < 1024; i += 256) a[i >> 6][i & 63] = A[(size_t)(m0 + (i >> 6)) * K + k0 + (i & 63)];
    __syncthreads();
    if (n < N) for (int k = 0; k < 64; ++k) { float w = b2f(W[(size_t)(k0 + k) * N + n]);
#pragma unroll
      for (int i = 0; i < 16; ++i) acc[i] += a[i][k] * w; }
  }
  if (n < N) for (int i = 0; i < 16; ++i) C[(size_t)(m0 + i) * N + n] = relu ? fmaxf(acc[i], 0.f) : acc[i];
}
__global__ void attn(const float* qkv, float* o) {
  int h = blockIdx.y, q = blockIdx.x * 256 + threadIdx.x;
  float qv[DH], acc[DH];
#pragma unroll
  for (int d = 0; d < DH; ++d) { qv[d] = qkv[(size_t)q * 1920 + h * DH + d] * 0.22360679774997896f; acc[d] = 0; }
  float m = -1e30f, l = 0;
  for (int k = 0; k < S; ++k) {
    const float* kp = qkv + (size_t)k * 1920 + 640 + h * DH; const float* vp = kp + 640;
    float s = 0;
#pragma unroll
    for (int d = 0; d < DH; ++d) s += qv[d] * kp[d];
    float mn = fmaxf(m, s), al = __expf(m - mn), p = __expf(s - mn); l = l * al + p; m = mn;
#pragma unroll
    for (int d = 0; d < DH; ++d) acc[d] = acc[d] * al + p * vp[d];
  }
#pragma unroll
  for (int d = 0; d < DH; ++d) o[(size_t)q * D + h * DH + d] = acc[d] / l;
}
__global__ void fin(const unsigned short* x, const float* s, const float* g, __hip_bfloat16* out) {
  int i = blockIdx.x * 256 + threadIdx.x; float gv = g[i]; out[i] = __float2bfloat16(b2f(x[i]) + s[i] * (gv / (1.f + __expf(-gv))));
}
struct Bufs { float *xn, *qkv, *h, *o, *s, *g; };
inline Bufs carve(void* ws) { Bufs b; float* p = (float*)ws; b.xn = p; p += S * D; b.qkv = p; p += S * 1920; b.h = p; p += S * FF; b.o = p; p += S * D; b.s = p; p += S * D; b.g = p; return b; }
inline void launch(void* const* d_in, void* d_out, void* ws, hipStream_t st) {
  auto x = (const unsigned short*)d_in[0]; auto wqkv = (const unsigned short*)d_in[1]; auto wo = (const unsigned short*)d_in[2];
  auto w1 = (const unsigned short*)d_in[4]; auto w2 = (const unsigned short*)d_in[5];
  Bufs b = carve(ws);
  ln<<<S, 64, 0, st>>>(x, b.xn);
  gemm<<<dim3(8, S / 16), 256, 0, st>>>(b.xn, wqkv, b.qkv, D, 1920, 0);
  gemm<<<dim3(6, S / 16), 256, 0, st>>>(b.xn, w1, b.h, D, FF, 1);
  attn<<<dim3(S / 256, H), 256, 0, st>>>(b.qkv, b.o);
  gemm<<<dim3(3, S / 16), 256, 0, st>>>(b.o, wo, b.s, D, D, 0);
  gemm<<<dim3(3, S / 16), 256, 0, st>>>(b.h, w2, b.g, FF, D, 0);
  fin<<<S * D / 256, 256, 0, st>>>(x, b.s, b.g, (__hip_bfloat16*)d_out);
}
}
extern "C" void kernel_launch(void* const* d_in, const int* in_sizes, int n_in, void* d_out, int out_size, void* d_ws, size_t ws_size, hipStream_t stream) {
  ref::launch(d_in, d_out, d_ws, stream);
}
```

```cpp
#include <hip/hip_runtime.h>
#include <hip/hip_bf16.h>
#include <stdint.h>
#include <utility>

namespace {
typedef __attribute__((ext_vector_type(2))) unsigned u32x2;
typedef __attribute__((ext_vector_type(4))) unsigned u32x4;
typedef __attribute__((ext_vector_type(16))) unsigned u32x16;
typedef __attribute__((ext_vector_type(16))) float f32x16;
typedef __attribute__((ext_vector_type(8))) __bf16 bf16x8;
typedef __attribute__((ext_vector_type(4))) short s16x4;
typedef __amdgpu_buffer_rsrc_t rsrc_t;

constexpr unsigned WS_F1 = 0x10000, WS_STATS = 0x18000, WS_DBG = 0x20000, WS_F3 = 0xf0000;
constexpr unsigned PS = 163840 + 512, HS = 98304 + 512, KS = 20480 + 512, OS = 40960 + 512;
constexpr unsigned WS_QKV = 0x100000, QKV_SEC = 16 * PS;
constexpr unsigned WS_H = WS_QKV + 3 * QKV_SEC;
constexpr unsigned WS_WT = WS_H + 64 * HS;
constexpr unsigned WS_O = WS_WT + 96 * KS;
constexpr unsigned WS_END = WS_O + 64 * OS;
constexpr unsigned OOB = 0x80000000u;
constexpr unsigned LDS_W = 81920, LDS_KV = 122880, LDS_CS = 160256, LDS_ML = 161792, LDS_BYTES = 163840;
constexpr int SC1 = 16;

enum { C_gt0, C_gt1, C_dA0, C_dD0, C_dA1, C_dD1, C_dA2, C_dD2, C_dA3, C_dD3, C_qs, C_sst, C_sso, C_sh0, C_sh1, C_swt, C_dE1, C_dE3, C_xb, C_wb, C_csr, C_csw, C_wta, C_so, C_hodd, C_kvb, C_qsm, C_qsr, C_qkm, C_qkr, C_kf, C_mw, C_mr, C_ml, C_obm, C_obr, C_f3, C_head, C_cso, C_sx0, C_sx2, C_vx, C_poll, C_ap, C_kh32, C_aw, C_ar, C_ag };
enum { H_o1, H_sq, H_sqr, H_sk0, H_sk0r, H_skv, H_srm, H_sst, H_shb, H_shs, H_sp0, H_sp1, H_sp2, H_swo, H_v1b, H_vob, H_half, H_fl, H_pollb, H_ep, H_wtb, H_m1, H_mkv, H_mst, H_mwt, H_mwo, H_g0, H_g1, H_g3, H_lx };
struct Rows { unsigned v[12][48]; };
constexpr Rows make_rows(unsigned b) {
    Rows R = {};
    const unsigned xcd = b & 7, jj = b >> 3;
    const unsigned rb = (xcd & 1) * 8 + (jj & 7), cb = (xcd >> 1) * 4 + (jj >> 3);
    const unsigned head = xcd * 4 + (jj >> 3), qb = jj & 7;
    const unsigned half = jj & 1, ttb = 2 * head + half, tts = ttb ^ 1;
    const unsigned c0 = 5 * ((jj & 7) >> 1) + 2 * half, p0 = 2 * half, p2 = 2 - p0;
    const unsigned G[5] = {0x74653210u, 0x76543210u, 0x76543210u, 0x50764321u, 0x72654310u};
    const unsigned H[5][4] = {{0, 1, 4, 2}, {5, 8, 9, 10}, {12, 13, 16, 17}, {20, 21, 24, 18}, {25, 28, 29, 26}};
    for (unsigned w = 0; w < 8; ++w) {
        unsigned* c = R.v[w];
        const unsigned wm = w & 3, wn = w >> 2, tt = 4 * rb + wm, qt = 8 * qb + 2 * wm + wn;
        const unsigned wr = wm & 1, wc = wm >> 1;
        for (unsigned i = 0; i < 2; ++i) {
            const unsigned T = 4 * cb + 2 * wc + i, sec = T / 20, ty = T % 5, tq = 4 * rb + 2 * wr; unsigned d[4] = {};
            for (unsigned s = 0; s < 4; ++s) {
                const unsigned hd = (T % 20) / 5 * 8 + (H[ty][s] >> 2), pl = H[ty][s] & 3;
                d[s] = T >= 60 ? OOB : WS_QKV + sec * QKV_SEC + (hd >> 1) * PS + (pl < 2 ? (hd & 1) * 65536 + tq * 1024 + pl * 512 : 131072 + tq * 512) + (sec == 2 ? pl * 64 : 0);
            }
            c[C_gt0 + i] = G[ty]; c[C_dA0 + 4 * i] = d[0]; c[C_dD0 + 4 * i] = d[0] ^ d[1]; c[C_dA1 + 4 * i] = d[2]; c[C_dD1 + 4 * i] = d[2] ^ d[3];
            c[C_dE1 + i] = (d[2] + 1024) ^ (d[3] + (ty == 2 ? 1024 : 512));
        }
        c[C_qs] = cb < 5 ? 0x3ea52b5bu : 0x3f800000u;
        c[C_sst] = WS_STATS + (128 * rb + 8 * cb + w) * 16; c[C_sso] = WS_STATS + 2048 * rb - LDS_W;
        const unsigned unit = wm * 256 + b, sh = WS_H + tt * HS + 6 * cb * 1024;
        c[C_sh0] = wn ? sh : OOB; c[C_sh1] = wn ? sh + 2048 : OOB; c[C_swt] = wn ? sh + 4096 : unit < 960 ? WS_WT + unit / 10 * KS + unit % 10 * 2048 : OOB;
        c[C_xb] = wn ? wm * 20480 + wm * 32 : wr * 40960 + wr * 64; c[C_wb] = LDS_W + wr * 8192 + wc * 128;
        c[C_csr] = LDS_CS + (wn ? 2048 : wc * 1024); c[C_csw] = c[C_csr] + (wn ? wm : wr) * 128; c[C_wta] = LDS_W + 65536 + wm * 2048; c[C_so] = LDS_W + (wn ? wm * 512 : wr * 1024);
        c[C_hodd] = (head & 1) * 8; c[C_kvb] = LDS_KV + wn * 3072;
        const unsigned qo = wm * 20480 + 0;
        c[C_qsm] = qo + (wn ^ 1) * 1024; c[C_qsr] = qo + 2048 + (wn ^ 1) * 512; c[C_qkm] = qo + wn * 1024; c[C_qkr] = qo + 2048 + wn * 512; c[C_kf] = wn * 20480 + 4096;
        c[C_mw] = LDS_KV + 12352 + w * 3072; c[C_mr] = LDS_KV + 12352 + (w ^ 4) * 3072; c[C_ml] = LDS_ML + w * 256;
        const unsigned ob = WS_O + qt * OS + (head >> 4) * 20480;
        c[C_obm] = ob + (head & 15) * 1024; c[C_obr] = ob + 16384 + (head & 15) * 256; c[C_head] = head;
        c[C_f3] = WS_F3 + (qt * 8 + (head >> 4) * 4 + (head & 3)) * 128 + ((head >> 2) & 3) * 8;
        const unsigned r = wm >> 1, kh = wm & 1, ct = r ? tts : ttb, xb = LDS_KV + (r ? 12288 : 0), xs = r ? 4096 : 6144;
        c[C_cso] = WS_O + ct * OS + kh * 20480; c[C_sx0] = p0 * 64; c[C_sx2] = p2 * 64; c[C_vx] = 32 * ct * 1280 + (32 * c0 + 16 * kh) * 2; c[C_poll] = WS_F3 + (ct * 8 + 4 * kh) * 128;
        c[C_ap] = kh * 61440; c[C_kh32] = kh * 32; c[C_aw] = xb + kh * xs; c[C_ar] = xb + (kh ^ 1) * xs; c[C_ag] = LDS_KV + 20480 + (kh ^ 1) * 5120 + 3072 * r;
    }
    for (unsigned w = 0; w < 4; ++w) {
        unsigned* h = R.v[8 + w];
        h[H_o1] = (5 * ((jj ^ (jj >> 3)) & 1) + (xcd >> 1) + 2 * (xcd & 1)) % 10 * (64 * 3072);
        const unsigned pb = WS_QKV + (head >> 1) * PS, pm = pb + (head & 1) * 65536, pr = pb + 131072, k0 = pm + QKV_SEC + (w & 1) * 32768, k0r = pr + QKV_SEC + (w & 1) * 16384;
        h[H_sq] = pm + (8 * qb + 2 * w) * 1024; h[H_sqr] = pr + (8 * qb + 2 * w) * 512 - 2048; h[H_sk0] = k0; h[H_sk0r] = k0r - 1024;
        h[H_skv] = k0 + (w < 2 ? 1024 : QKV_SEC); h[H_srm] = k0r - 2048 + (w < 2 ? 512 : QKV_SEC); h[H_sst] = WS_STATS + (128 * rb + 32 * w) * 16;
        h[H_shb] = WS_H + ttb * HS + 24 * w * 1024; h[H_shs] = WS_H + tts * HS + 24 * w * 1024;
        const unsigned sw2 = WS_WT + 24 * w * KS + c0 * 1024, unit = w * 256 + b;
        h[H_sp0] = sw2 + p0 * 1024; h[H_sp1] = sw2 + 1024; h[H_sp2] = sw2 + p2 * 1024; h[H_swo] = w * 204800 + c0 * 64;
        h[H_v1b] = 16 * w * 3072 + cb * 192;
        h[H_fl] = w == 0 ? WS_F1 + b * 8 : OOB; h[H_pollb] = WS_F1 + xcd * 2048; h[H_ep] = b == 0 && w == 0 ? 0 : OOB; h[H_wtb] = unit / 10 * 20480 + unit % 10 * 128;
        h[H_m1] = LDS_W + w * 3072; h[H_mkv] = LDS_KV + w * 3072; h[H_mst] = LDS_W + w * 512; h[H_mwt] = LDS_W + 65536 + w * 2048; h[H_mwo] = w * 30720;
        h[H_g0] = LDS_KV + w * 5120; h[H_g1] = LDS_KV + (w ^ 1) * 5120; h[H_g3] = LDS_KV + (w ^ 3) * 5120;
        h[H_lx] = (16 * (w & 1) + 8 * (w >> 1)) * 16;
    }
    return R;
}
template <unsigned B> struct Blk { static constexpr Rows v = make_rows(B); };
struct Table { Rows r[256]; };
template <size_t... B> constexpr Table make_table(std::index_sequence<B...>) { return Table{{Blk<B>::v...}}; }
__device__ const Table TAB = make_table(std::make_index_sequence<256>{});

#define MFMA16(a, b, c) __builtin_amdgcn_mfma_f32_32x32x16_bf16(__builtin_bit_cast(bf16x8, a), __builtin_bit_cast(bf16x8, b), c, 0, 0, 0)
#define MFMA8(a, b, c) __builtin_amdgcn_mfma_f32_32x32x8bf16_1k(__builtin_bit_cast(s16x4, a), __builtin_bit_cast(s16x4, b), c, 0, 0, 0)
__device__ __forceinline__ unsigned pk(float lo, float hi) { typedef __attribute__((ext_vector_type(2))) __bf16 b2; b2 v = {(__bf16)lo, (__bf16)hi}; return __builtin_bit_cast(unsigned, v); }

__device__ __forceinline__ f32x16 attn_exact(rsrc_t rws, unsigned head, u32x4 qm, u32x2 qr, int lane) {
    const int hi = lane >> 5, d = lane & 31, pl = d < 16 ? d >> 3 : 2;
    const unsigned pb = WS_QKV + QKV_SEC + (head >> 1) * PS, km0 = pb + (head & 1) * 65536, kr0 = pb + 131072 + (head & 1) * 8;
    const unsigned vb0 = QKV_SEC + (d < 16 ? km0 + pl * 512 + (d & 7) * 2 : kr0 + (d & 3) * 2), cst = (d & 3) ? 0u : 0x3f803f80u;
    if (hi) qr = (u32x2){0u, 0u};
    f32x16 o = {}; float m = -INFINITY;
#pragma unroll 1
    for (int t = 0; t < 64; ++t) {
        const unsigned vb = vb0 + t * (d < 16 ? 1024 : 512);
        const u32x4 km = __builtin_amdgcn_raw_buffer_load_b128(rws, km0 + t * 1024 + lane * 16, 0, SC1);
        const u32x2 kr = __builtin_amdgcn_raw_buffer_load_b64(rws, kr0 + t * 512 + d * 16, 0, SC1);
        f32x16 s = MFMA16(km, qm, (f32x16){}); s = MFMA8(kr, qr, s);
        float mn = m;
#pragma unroll
        for (int r = 0; r < 16; ++r) mn = fmaxf(mn, s[r]);
        { auto r = __builtin_amdgcn_permlane32_swap(__float_as_uint(mn), __float_as_uint(mn), false, false); mn = fmaxf(__uint_as_float(r[0]), __uint_as_float(r[1])); }
        const float mz = mn == -INFINITY ? 0.f : mn, al = __builtin_amdgcn_exp2f(m - mz); m = mn;
#pragma unroll
        for (int r = 0; r < 16; ++r) { o[r] *= al; s[r] = __builtin_amdgcn_exp2f(s[r] - mz); }
#pragma unroll
        for (int s2 = 0; s2 < 2; ++s2) {
            u32x4 vf, pf;
#pragma unroll
            for (int j = 0; j < 4; ++j) {
                const int key = 16 * s2 + 8 * (j >> 1) + 4 * hi + 2 * (j & 1);
                const unsigned lo = __builtin_amdgcn_raw_buffer_load_b16(rws, vb + (key ^ (4 * pl)) * 16, 0, SC1), up = __builtin_amdgcn_raw_buffer_load_b16(rws, vb + ((key + 1) ^ (4 * pl)) * 16, 0, SC1);
                vf[j] = d < 20 ? lo | up << 16 : cst; pf[j] = pk(s[8 * s2 + 2 * j], s[8 * s2 + 2 * j + 1]);
            }
            o = MFMA16(vf, pf, o);
        }
    }
    asm volatile("s_nop 15" : "+v"(o));
    return o;
}

__global__ __launch_bounds__(768) void fused_block(const void* x, unsigned n0, unsigned f0, const void* wqkv, unsigned n1, unsigned f1, void* ws, unsigned n2, unsigned f2, const void* w2, unsigned n3,
                                                   unsigned f3, const void* w1, unsigned n4, unsigned f4, const void* wo, unsigned n5, unsigned f5, void* out, unsigned n6, unsigned f6) {
    __shared__ __attribute__((aligned(16))) char smem[LDS_BYTES];
    f32x16 o; u32x16 t2; u32x4 qm, rx, rw, ro; u32x2 qr, bad, tag; unsigned l16, wid, dbg; const void* tab = &TAB; auto ka = __builtin_amdgcn_kernarg_segment_ptr();
    asm volatile(
  "s_load_dwordx16 s[8:23], s[0:1], 0x0\n"
  "s_load_dwordx8 s[24:31], s[0:1], 0x40\n"
  "s_load_dwordx4 s[32:35], s[0:1], 0x60\n"
  "v_readfirstlane_b32 s3, v0\n"
  "s_getpc_b64 s[36:37]\n"
  "s_lshr_b32 s44, s2, 3\n"
  "s_mov_b32 s38, 33280\n"
  "s_mov_b32 s39, 0x20000\n"
  "s_lshl_b32 s40, s44, 7\n"
  "v_lshl_add_u32 v1, v0, 12, s40\n"
  "s_and_b32 s37, s37, 0xffff\n"
  "v_and_b32 v0, 63, v0\n"
  "s_lshr_b32 s3, s3, 6\n"
  "buffer_load_dword v89, v1, s[36:39], 0 offen\n"
  "s_and_b32 s45, s2, 1\n"
  "s_lshl_b32 s45, s45, 3\n"
  "s_and_b32 s40, s44, 7\n"
  "s_or_b32 s45, s45, s40\n"
  "s_and_b32 s41, s2, 6\n"
  "s_lshl_b32 s41, s41, 1\n"
  "s_lshr_b32 s43, s2, 6\n"
  "s_or_b32 s41, s41, s43\n"
  "s_and_b32 s4, s3, 3\n"
  "s_lshr_b32 s5, s3, 2\n"
  "s_mul_i32 s40, s2, 12\n"
  "s_add_u32 s40, s40, s3\n"
  "s_mul_i32 s40, s40, 192\n"
  "s_add_u32 s6, s6, s40\n"
  "s_addc_u32 s7, s7, 0\n"
  "s_mul_i32 s50, s4, 20480\n"
  "s_lshl_b32 s51, s4, 12\n"
  "s_add_u32 s51, s51, 81920\n"
  "s_cmp_ge_u32 s3, 8\n"
  "s_cbranch_scc1 51f\n"
  "s_lshl_b32 s40, s45, 4\n"
  "s_add_u32 s40, s40, s41\n"
  "s_lshl_b32 s40, s40, 3\n"
  "s_add_u32 s40, s40, s3\n"
  "s_mul_i32 s40, s40, 1280\n"
  "v_lshlrev_b32 v1, 2, v0\n"
  "v_lshlrev_b32 v156, 4, v0\n"
  "s_waitcnt lgkmcnt(0)\n"
  "buffer_load_dword v84, v1, s[8:11], s40 offen offset:0\n"
  "buffer_load_dword v85, v1, s[8:11], s40 offen offset:256\n"
  "buffer_load_dword v86, v1, s[8:11], s40 offen offset:512\n"
  "buffer_load_dword v87, v1, s[8:11], s40 offen offset:768\n"
  "buffer_load_dword v88, v1, s[8:11], s40 offen offset:1024\n"
  "buffer_load_dwordx2 v[90:91], off, s[16:19], 0 sc1\n"
  "s_barrier\n"
  "s_load_dwordx16 s[52:67], s[6:7], 0x0\n"
  "s_load_dwordx16 s[68:83], s[6:7], 0x40\n"
  "s_load_dwordx16 s[84:99], s[6:7], 0x80\n"
  "s_branch 50f\n"
  "51:\n"
  "s_setprio 3\n"
  "v_lshrrev_b32 v2, 3, v0\n"
  "v_lshrrev_b32 v3, 4, v0\n"
  "v_lshlrev_b32 v160, 4, v0\n"
  "s_lshl_b32 s40, s45, 2\n"
  "s_add_u32 s40, s40, s4\n"
  "s_mul_i32 s40, s40, 40960\n"
  "v_mul_u32_u24 v95, 0x500, v2\n"
  "v_xor_b32 v4, v0, v3\n"
  "v_and_b32 v4, 7, v4\n"
  "v_add_u32 v2, s40, v95\n"
  "v_lshl_or_b32 v96, v4, 4, v2\n"
  "s_lshl_b32 s40, s41, 8\n"
  "s_cmp_lt_u32 s41, 15\n"
  "s_cselect_b32 s40, s40, 0x80000000\n"
  "s_mul_i32 s42, s4, 61440\n"
  "s_add_u32 s40, s40, s42\n"
  "v_mul_u32_u24 v2, 0xf00, v3\n"
  "v_and_b32 v4, 0xf0, v160\n"
  "v_lshlrev_b32 v3, 6, v3\n"
  "v_xor_b32 v4, v4, v3\n"
  "v_add3_u32 v100, v2, v4, s40\n"
  "v_xor_b32 v97, 64, v96\n"
  "v_add_u32 v98, 0x4800, v96\n"
  "v_add_u32 v99, 0x6c00, v97\n"
  "v_add_u32 v97, 0x2400, v97\n"
  "v_add_u32 v101, 0x3800, v100\n"
  "v_add_u32 v102, 0x7000, v100\n"
  "v_add_u32 v103, 0xa800, v100\n"
  "v_lshl_add_u32 v94, v0, 4, s50\n"
  "v_and_b32 v4, 0x70, v160\n"
  "v_add_u32 v95, v95, v4\n"
  "s_xor_b32 s40, s44, s43\n"
  "s_and_b32 s40, s40, 1\n"
  "s_mul_i32 s40, s40, 5\n"
  "s_bfe_u32 s42, s2, 0x20001\n"
  "s_add_u32 s40, s40, s42\n"
  "s_and_b32 s42, s2, 1\n"
  "s_lshl1_add_u32 s40, s42, s40\n"
  "s_cmp_eq_u32 s40, 10\n"
  "s_cselect_b32 s40, 0, s40\n"
  "s_lshl_b32 s48, s40, 7\n"
  "s_mul_i32 s49, s40, 245760\n"
  "s_setprio 0\n"
  "s_waitcnt lgkmcnt(0)\n"
  "s_barrier\n"
  "s_add_u32 m0, s50, 0\n"
  "s_add_u32 s40, s48, 0\n"
  "s_sub_u32 s41, s40, 1280\n"
  "s_cselect_b32 s40, s40, s41\n"
  "buffer_load_dwordx4 v96, s[8:11], s40 offen offset:0 lds\n"
  "buffer_load_dwordx4 v97, s[8:11], s40 offen offset:1024 lds\n"
  "buffer_load_dwordx4 v98, s[8:11], s40 offen offset:2048 lds\n"
  "buffer_load_dwordx4 v99, s[8:11], s40 offen offset:3072 lds\n"
  "s_load_dwordx2 s[46:47], s[16:17], 0x0\n"
  "s_load_dwordx16 s[52:67], s[6:7], 0x0\n"
  "s_load_dwordx16 s[68:83], s[6:7], 0x40\n"
  "s_add_u32 m0, s51, 32768\n"
  "s_add_u32 s40, s49, 0\n"
  "s_sub_u32 s41, s40, 2457600\n"
  "s_cselect_b32 s40, s40, s41\n"
  "buffer_load_dwordx4 v100, s[12:15], s40 offen offset:0 lds\n"
  "buffer_load_dwordx4 v101, s[12:15], s40 offen offset:1024 lds\n"
  "buffer_load_dwordx4 v102, s[12:15], s40 offen offset:2048 lds\n"
  "buffer_load_dwordx4 v103, s[12:15], s40 offen offset:3072 lds\n"
  "s_bitcmp1_b32 s2, 3\n"
  "s_cselect_b64 vcc, -1, 0\n"
  "v_and_b32 v85, 48, v160\n"
  "v_bfrev_b32 v107, 1\n"
  "v_bfrev_b32 v108, 1\n"
  "v_mov_b32 v81, 160000\n"
  "v_mov_b32 v82, 0\n"
  "ds_write_b32 v81, v82\n"
  "v_add_u32 v81, 0, v0\n"
  "v_mul_u32_u24 v82, 0xab, v81\n"
  "v_lshrrev_b32 v82, 11, v82\n"
  "v_mad_i32_i24 v81, v82, -12, v81\n"
  "v_mul_u32_u24 v83, 0xc00, v82\n"
  "v_lshl_add_u32 v83, v81, 4, v83\n"
  "v_subrev_u32 v104, 0, v83\n"
  "v_lshrrev_b32 v81, 2, v81\n"
  "v_sub_u32 v83, 2, v81\n"
  "v_cndmask_b32 v81, v81, v83, vcc\n"
  "v_mul_u32_u24 v82, 0x500, v82\n"
  "v_lshl_add_u32 v82, v81, 6, v82\n"
  "v_subrev_u32 v82, 0, v82\n"
  "v_add_u32 v162, v82, v85\n"
  "v_add_u32 v81, 64, v0\n"
  "v_mul_u32_u24 v82, 0xab, v81\n"
  "v_lshrrev_b32 v82, 11, v82\n"
  "v_mad_i32_i24 v81, v82, -12, v81\n"
  "v_mul_u32_u24 v83, 0xc00, v82\n"
  "v_lshl_add_u32 v83, v81, 4, v83\n"
  "v_subrev_u32 v105, 1024, v83\n"
  "v_lshrrev_b32 v81, 2, v81\n"
  "v_sub_u32 v83, 2, v81\n"
  "v_cndmask_b32 v81, v81, v83, vcc\n"
  "v_mul_u32_u24 v82, 0x500, v82\n"
  "v_lshl_add_u32 v82, v81, 6, v82\n"
  "v_subrev_u32 v82, 1024, v82\n"
  "v_add_u32 v163, v82, v85\n"
  "v_add_u32 v81, 128, v0\n"
  "v_mul_u32_u24 v82, 0xab, v81\n"
  "v_lshrrev_b32 v82, 11, v82\n"
  "v_mad_i32_i24 v81, v82, -12, v81\n"
  "v_mul_u32_u24 v83, 0xc00, v82\n"
  "v_lshl_add_u32 v83, v81, 4, v83\n"
  "v_subrev_u32 v106, 2048, v83\n"
  "v_lshrrev_b32 v81, 2, v81\n"
  "v_sub_u32 v83, 2, v81\n"
  "v_cndmask_b32 v81, v81, v83, vcc\n"
  "v_mul_u32_u24 v82, 0x500, v82\n"
  "v_lshl_add_u32 v82, v81, 6, v82\n"
  "v_subrev_u32 v82, 2048, v82\n"
  "v_add_u32 v164, v82, v85\n"
  "s_add_u32 m0, s50, 4096\n"
  "s_add_u32 s40, s48, 128\n"
  "s_sub_u32 s41, s40, 1280\n"
  "s_cselect_b32 s40, s40, s41\n"
  "buffer_load_dwordx4 v96, s[8:11], s40 offen offset:0 lds\n"
  "buffer_load_dwordx4 v97, s[8:11], s40 offen offset:1024 lds\n"
  "buffer_load_dwordx4 v98, s[8:11], s40 offen offset:2048 lds\n"
  "buffer_load_dwordx4 v99, s[8:11], s40 offen offset:3072 lds\n"
  "s_add_u32 m0, s51, 49152\n"
  "s_add_u32 s40, s49, 245760\n"
  "s_sub_u32 s41, s40, 2457600\n"
  "s_cselect_b32 s40, s40, s41\n"
  "buffer_load_dwordx4 v100, s[12:15], s40 offen offset:0 lds\n"
  "buffer_load_dwordx4 v101, s[12:15], s40 offen offset:1024 lds\n"
  "buffer_load_dwordx4 v102, s[12:15], s40 offen offset:2048 lds\n"
  "buffer_load_dwordx4 v103, s[12:15], s40 offen offset:3072 lds\n"
  "s_setprio 3\n"
  "s_waitcnt lgkmcnt(0)\n"
  "s_add_u32 s46, s46, 1\n"
  "s_add_u32 s47, s47, 1\n"
  "v_add_u32 v104, s66, v104\n"
  "v_add_u32 v105, s66, v105\n"
  "v_add_u32 v106, s66, v106\n"
  "v_lshl_add_u32 v109, v0, 5, s70\n"
  "v_xor_b32 v161, s81, v160\n"
  "v_mov_b32 v110, s46\n"
  "v_mov_b32 v111, s47\n"
  "s_mov_b64 exec, 0xff\n"
  "v_lshl_add_u32 v108, v0, 11, s69\n"
  "s_mov_b64 exec, 1\n"
  "v_mov_b32 v107, s71\n"
  "s_mov_b64 exec, -1\n"
  "s_waitcnt vmcnt(8)\n"
  "s_barrier\n"
  "ds_read_b128 v[0:3], v94 offset:0\n"
  "ds_read_b128 v[4:7], v94 offset:1024\n"
  "ds_read_b128 v[8:11], v94 offset:2048\n"
  "ds_read_b128 v[12:15], v94 offset:3072\n"
  "s_add_u32 m0, s50, 8192\n"
  "s_add_u32 s40, s48, 256\n"
  "s_sub_u32 s41, s40, 1280\n"
  "s_cselect_b32 s40, s40, s41\n"
  "buffer_load_dwordx4 v96, s[8:11], s40 offen offset:0 lds\n"
  "buffer_load_dwordx4 v97, s[8:11], s40 offen offset:1024 lds\n"
  "buffer_load_dwordx4 v98, s[8:11], s40 offen offset:2048 lds\n"
  "buffer_load_dwordx4 v99, s[8:11], s40 offen offset:3072 lds\n"
  "s_add_u32 m0, s51, 0\n"
  "s_add_u32 s40, s49, 491520\n"
  "s_sub_u32 s41, s40, 2457600\n"
  "s_cselect_b32 s40, s40, s41\n"
  "buffer_load_dwordx4 v100, s[12:15], s40 offen offset:0 lds\n"
  "buffer_load_dwordx4 v101, s[12:15], s40 offen offset:1024 lds\n"
  "buffer_load_dwordx4 v102, s[12:15], s40 offen offset:2048 lds\n"
  "buffer_load_dwordx4 v103, s[12:15], s40 offen offset:3072 lds\n"
  "s_mov_b32 m0, s76\n"
  "s_add_u32 s40, s72, 9216\n"
  "buffer_load_dwordx4 v95, s[20:23], s72 offen lds\n"
  "buffer_load_dwordx4 v95, s[20:23], s40 offen offset:1024 lds\n"
  "s_waitcnt vmcnt(10) lgkmcnt(0)\n"
  "s_barrier\n"
  "ds_read_b128 v[16:19], v94 offset:4096\n"
  "ds_read_b128 v[20:23], v94 offset:5120\n"
  "ds_read_b128 v[24:27], v94 offset:6144\n"
  "ds_read_b128 v[28:31], v94 offset:7168\n"
  "s_add_u32 m0, s50, 12288\n"
  "s_add_u32 s40, s48, 384\n"
  "s_sub_u32 s41, s40, 1280\n"
  "s_cselect_b32 s40, s40, s41\n"
  "buffer_load_dwordx4 v96, s[8:11], s40 offen offset:0 lds\n"
  "buffer_load_dwordx4 v97, s[8:11], s40 offen offset:1024 lds\n"
  "buffer_load_dwordx4 v98, s[8:11], s40 offen offset:2048 lds\n"
  "buffer_load_dwordx4 v99, s[8:11], s40 offen offset:3072 lds\n"
  "s_add_u32 m0, s51, 16384\n"
  "s_add_u32 s40, s49, 737280\n"
  "s_sub_u32 s41, s40, 2457600\n"
  "s_cselect_b32 s40, s40, s41\n"
  "buffer_load_dwordx4 v100, s[12:15], s40 offen offset:0 lds\n"
  "buffer_load_dwordx4 v101, s[12:15], s40 offen offset:1024 lds\n"
  "buffer_load_dwordx4 v102, s[12:15], s40 offen offset:2048 lds\n"
  "buffer_load_dwordx4 v103, s[12:15], s40 offen offset:3072 lds\n"
  "s_waitcnt vmcnt(10) lgkmcnt(0)\n"
  "s_barrier\n"
  "ds_read_b128 v[32:35], v94 offset:8192\n"
  "ds_read_b128 v[36:39], v94 offset:9216\n"
  "ds_read_b128 v[40:43], v94 offset:10240\n"
  "ds_read_b128 v[44:47], v94 offset:11264\n"
  "s_add_u32 m0, s50, 16384\n"
  "s_add_u32 s40, s48, 512\n"
  "s_sub_u32 s41, s40, 1280\n"
  "s_cselect_b32 s40, s40, s41\n"
  "buffer_load_dwordx4 v96, s[8:11], s40 offen offset:0 lds\n"
  "buffer_load_dwordx4 v97, s[8:11], s40 offen offset:1024 lds\n"
  "buffer_load_dwordx4 v98, s[8:11], s40 offen offset:2048 lds\n"
  "buffer_load_dwordx4 v99, s[8:11], s40 offen offset:3072 lds\n"
  "s_add_u32 m0, s51, 32768\n"
  "s_add_u32 s40, s49, 983040\n"
  "s_sub_u32 s41, s40, 2457600\n"
  "s_cselect_b32 s40, s40, s41\n"
  "buffer_load_dwordx4 v100, s[12:15], s40 offen offset:0 lds\n"
  "buffer_load_dwordx4 v101, s[12:15], s40 offen offset:1024 lds\n"
  "buffer_load_dwordx4 v102, s[12:15], s40 offen offset:2048 lds\n"
  "buffer_load_dwordx4 v103, s[12:15], s40 offen offset:3072 lds\n"
  "s_waitcnt vmcnt(8) lgkmcnt(0)\n"
  "s_barrier\n"
  "ds_read_b128 v[48:51], v94 offset:12288\n"
  "ds_read_b128 v[52:55], v94 offset:13312\n"
  "ds_read_b128 v[56:59], v94 offset:14336\n"
  "ds_read_b128 v[60:63], v94 offset:15360\n"
  "s_add_u32 m0, s50, 0\n"
  "s_add_u32 s40, s48, 640\n"
  "s_sub_u32 s41, s40, 1280\n"
  "s_cselect_b32 s40, s40, s41\n"
  "buffer_load_dwordx4 v96, s[8:11], s40 offen offset:0 lds\n"
  "buffer_load_dwordx4 v97, s[8:11], s40 offen offset:1024 lds\n"
  "buffer_load_dwordx4 v98, s[8:11], s40 offen offset:2048 lds\n"
  "buffer_load_dwordx4 v99, s[8:11], s40 offen offset:3072 lds\n"
  "s_add_u32 m0, s51, 49152\n"
  "s_add_u32 s40, s49, 1228800\n"
  "s_sub_u32 s41, s40, 2457600\n"
  "s_cselect_b32 s40, s40, s41\n"
  "buffer_load_dwordx4 v100, s[12:15], s40 offen offset:0 lds\n"
  "buffer_load_dwordx4 v101, s[12:15], s40 offen offset:1024 lds\n"
  "buffer_load_dwordx4 v102, s[12:15], s40 offen offset:2048 lds\n"
  "buffer_load_dwordx4 v103, s[12:15], s40 offen offset:3072 lds\n"
  "s_waitcnt vmcnt(8) lgkmcnt(0)\n"
  "s_barrier\n"
  "ds_read_b128 v[64:67], v94 offset:16384\n"
  "ds_read_b128 v[68:71], v94 offset:17408\n"
  "ds_read_b128 v[72:75], v94 offset:18432\n"
  "ds_read_b128 v[76:79], v94 offset:19456\n"
  "s_add_u32 m0, s50, 4096\n"
  "s_add_u32 s40, s48, 768\n"
  "s_sub_u32 s41, s40, 1280\n"
  "s_cselect_b32 s40, s40, s41\n"
  "buffer_load_dwordx4 v96, s[8:11], s40 offen offset:0 lds\n"
  "buffer_load_dwordx4 v97, s[8:11], s40 offen offset:1024 lds\n"
  "buffer_load_dwordx4 v98, s[8:11], s40 offen offset:2048 lds\n"
  "buffer_load_dwordx4 v99, s[8:11], s40 offen offset:3072 lds\n"
  "s_add_u32 m0, s51, 0\n"
  "s_add_u32 s40, s49, 1474560\n"
  "s_sub_u32 s41, s40, 2457600\n"
  "s_cselect_b32 s40, s40, s41\n"
  "buffer_load_dwordx4 v100, s[12:15], s40 offen offset:0 lds\n"
  "buffer_load_dwordx4 v101, s[12:15], s40 offen offset:1024 lds\n"
  "buffer_load_dwordx4 v102, s[12:15], s40 offen offset:2048 lds\n"
  "buffer_load_dwordx4 v103, s[12:15], s40 offen offset:3072 lds\n"
  "s_waitcnt vmcnt(8) lgkmcnt(0)\n"
  "s_barrier\n"
  "s_add_u32 m0, s50, 8192\n"
  "s_add_u32 s40, s48, 896\n"
  "s_sub_u32 s41, s40, 1280\n"
  "s_cselect_b32 s40, s40, s41\n"
  "buffer_load_dwordx4 v96, s[8:11], s40 offen offset:0 lds\n"
  "buffer_load_dwordx4 v97, s[8:11], s40 offen offset:1024 lds\n"
  "buffer_load_dwordx4 v98, s[8:11], s40 offen offset:2048 lds\n"
  "buffer_load_dwordx4 v99, s[8:11], s40 offen offset:3072 lds\n"
  "s_add_u32 m0, s51, 16384\n"
  "s_add_u32 s40, s49, 1720320\n"
  "s_sub_u32 s41, s40, 2457600\n"
  "s_cselect_b32 s40, s40, s41\n"
  "buffer_load_dwordx4 v100, s[12:15], s40 offen offset:0 lds\n"
  "buffer_load_dwordx4 v101, s[12:15], s40 offen offset:1024 lds\n"
  "buffer_load_dwordx4 v102, s[12:15], s40 offen offset:2048 lds\n"
  "buffer_load_dwordx4 v103, s[12:15], s40 offen offset:3072 lds\n"
  "s_waitcnt vmcnt(8)\n"
  "s_barrier\n"
  "s_add_u32 m0, s50, 12288\n"
  "s_add_u32 s40, s48, 1024\n"
  "s_sub_u32 s41, s40, 1280\n"
  "s_cselect_b32 s40, s40, s41\n"
  "buffer_load_dwordx4 v96, s[8:11], s40 offen offset:0 lds\n"
  "buffer_load_dwordx4 v97, s[8:11], s40 offen offset:1024 lds\n"
  "buffer_load_dwordx4 v98, s[8:11], s40 offen offset:2048 lds\n"
  "buffer_load_dwordx4 v99, s[8:11], s40 offen offset:3072 lds\n"
  "s_add_u32 m0, s51, 32768\n"
  "s_add_u32 s40, s49, 1966080\n"
  "s_sub_u32 s41, s40, 2457600\n"
  "s_cselect_b32 s40, s40, s41\n"
  "buffer_load_dwordx4 v100, s[12:15], s40 offen offset:0 lds\n"
  "buffer_load_dwordx4 v101, s[12:15], s40 offen offset:1024 lds\n"
  "buffer_load_dwordx4 v102, s[12:15], s40 offen offset:2048 lds\n"
  "buffer_load_dwordx4 v103, s[12:15], s40 offen offset:3072 lds\n"
  "s_waitcnt vmcnt(8)\n"
  "s_barrier\n"
  "s_add_u32 m0, s50, 16384\n"
  "s_add_u32 s40, s48, 1152\n"
  "s_sub_u32 s41, s40, 1280\n"
  "s_cselect_b32 s40, s40, s41\n"
  "buffer_load_dwordx4 v96, s[8:11], s40 offen offset:0 lds\n"
  "buffer_load_dwordx4 v97, s[8:11], s40 offen offset:1024 lds\n"
  "buffer_load_dwordx4 v98, s[8:11], s40 offen offset:2048 lds\n"
  "buffer_load_dwordx4 v99, s[8:11], s40 offen offset:3072 lds\n"
  "s_add_u32 m0, s51, 49152\n"
  "s_add_u32 s40, s49, 2211840\n"
  "s_sub_u32 s41, s40, 2457600\n"
  "s_cselect_b32 s40, s40, s41\n"
  "buffer_load_dwordx4 v100, s[12:15], s40 offen offset:0 lds\n"
  "buffer_load_dwordx4 v101, s[12:15], s40 offen offset:1024 lds\n"
  "buffer_load_dwordx4 v102, s[12:15], s40 offen offset:2048 lds\n"
  "buffer_load_dwordx4 v103, s[12:15], s40 offen offset:3072 lds\n"
  "s_waitcnt vmcnt(8)\n"
  "s_barrier\n"
  "s_add_u32 m0, s73, 65536\n"
  "s_add_u32 s40, s52, 983040\n"
  "s_sub_u32 s41, s40, 1966080\n"
  "s_cselect_b32 s40, s40, s41\n"
  "buffer_load_dwordx4 v104, s[24:27], s40 offen offset:0 lds\n"
  "buffer_load_dwordx4 v105, s[24:27], s40 offen offset:1024 lds\n"
  "buffer_load_dwordx4 v106, s[24:27], s40 offen offset:2048 lds\n"
  "s_add_u32 m0, s73, 4096\n"
  "s_add_u32 s40, s52, 1179648\n"
  "s_sub_u32 s41, s40, 1966080\n"
  "s_cselect_b32 s40, s40, s41\n"
  "buffer_load_dwordx4 v104, s[24:27], s40 offen offset:0 lds\n"
  "buffer_load_dwordx4 v105, s[24:27], s40 offen offset:1024 lds\n"
  "buffer_load_dwordx4 v106, s[24:27], s40 offen offset:2048 lds\n"
  "s_mov_b32 m0, s75\n"
  "s_mov_b32 exec_hi, 0\n"
  "buffer_load_dwordx4 v160, s[16:19], s59 offen sc1 lds\n"
  "s_mov_b64 exec, -1\n"
  "s_waitcnt vmcnt(7)\n"
  "s_barrier\n"
  "s_add_u32 m0, s73, 16384\n"
  "s_add_u32 s40, s52, 1376256\n"
  "s_sub_u32 s41, s40, 1966080\n"
  "s_cselect_b32 s40, s40, s41\n"
  "buffer_load_dwordx4 v104, s[24:27], s40 offen offset:0 lds\n"
  "buffer_load_dwordx4 v105, s[24:27], s40 offen offset:1024 lds\n"
  "buffer_load_dwordx4 v106, s[24:27], s40 offen offset:2048 lds\n"
  "s_waitcnt vmcnt(3)\n"
  "s_barrier\n"
  "s_add_u32 m0, s73, 28672\n"
  "s_add_u32 s40, s52, 1572864\n"
  "s_sub_u32 s41, s40, 1966080\n"
  "s_cselect_b32 s40, s40, s41\n"
  "buffer_load_dwordx4 v104, s[24:27], s40 offen offset:0 lds\n"
  "buffer_load_dwordx4 v105, s[24:27], s40 offen offset:1024 lds\n"
  "buffer_load_dwordx4 v106, s[24:27], s40 offen offset:2048 lds\n"
  "s_add_u32 m0, s73, 40960\n"
  "s_add_u32 s40, s52, 1769472\n"
  "s_sub_u32 s41, s40, 1966080\n"
  "s_cselect_b32 s40, s40, s41\n"
  "buffer_load_dwordx4 v104, s[24:27], s40 offen offset:0 lds\n"
  "buffer_load_dwordx4 v105, s[24:27], s40 offen offset:1024 lds\n"
  "buffer_load_dwordx4 v106, s[24:27], s40 offen offset:2048 lds\n"
  "s_add_u32 m0, s73, 53248\n"
  "s_add_u32 s40, s52, 0\n"
  "s_sub_u32 s41, s40, 1966080\n"
  "s_cselect_b32 s40, s40, s41\n"
  "buffer_load_dwordx4 v104, s[24:27], s40 offen offset:0 lds\n"
  "buffer_load_dwordx4 v105, s[24:27], s40 offen offset:1024 lds\n"
  "buffer_load_dwordx4 v106, s[24:27], s40 offen offset:2048 lds\n"
  "s_barrier\n"
  "s_add_u32 m0, s73, 65536\n"
  "s_add_u32 s40, s52, 196608\n"
  "s_sub_u32 s41, s40, 1966080\n"
  "s_cselect_b32 s40, s40, s41\n"
  "buffer_load_dwordx4 v104, s[24:27], s40 offen offset:0 lds\n"
  "buffer_load_dwordx4 v105, s[24:27], s40 offen offset:1024 lds\n"
  "buffer_load_dwordx4 v106, s[24:27], s40 offen offset:2048 lds\n"
  "s_waitcnt vmcnt(12)\n"
  "s_barrier\n"
  "s_sleep 1\n"
  "ds_write_b128 v94, v[0:3] offset:0\n"
  "s_sleep 1\n"
  "ds_write_b128 v94, v[4:7] offset:1024\n"
  "s_sleep 1\n"
  "ds_write_b128 v94, v[8:11] offset:2048\n"
  "s_sleep 1\n"
  "ds_write_b128 v94, v[12:15] offset:3072\n"
  "s_waitcnt vmcnt(9) lgkmcnt(0)\n"
  "s_barrier\n"
  "s_sleep 1\n"
  "ds_write_b128 v94, v[16:19] offset:4096\n"
  "s_sleep 1\n"
  "ds_write_b128 v94, v[20:23] offset:5120\n"
  "s_sleep 1\n"
  "ds_write_b128 v94, v[24:27] offset:6144\n"
  "s_sleep 1\n"
  "ds_write_b128 v94, v[28:31] offset:7168\n"
  "s_waitcnt vmcnt(6) lgkmcnt(0)\n"
  "s_barrier\n"
  "s_sleep 1\n"
  "ds_write_b128 v94, v[32:35] offset:8192\n"
  "s_sleep 1\n"
  "ds_write_b128 v94, v[36:39] offset:9216\n"
  "s_sleep 1\n"
  "ds_write_b128 v94, v[40:43] offset:10240\n"
  "s_sleep 1\n"
  "ds_write_b128 v94, v[44:47] offset:11264\n"
  "s_waitcnt vmcnt(3) lgkmcnt(0)\n"
  "s_barrier\n"
  "buffer_store_dwordx2 v[110:111], v108, s[16:19], 0 offen sc1\n"
  "s_add_u32 m0, s73, 4096\n"
  "s_add_u32 s40, s52, 393216\n"
  "s_sub_u32 s41, s40, 1966080\n"
  "s_cselect_b32 s40, s40, s41\n"
  "buffer_load_dwordx4 v104, s[24:27], s40 offen offset:0 lds\n"
  "buffer_load_dwordx4 v105, s[24:27], s40 offen offset:1024 lds\n"
  "buffer_load_dwordx4 v106, s[24:27], s40 offen offset:2048 lds\n"
  "s_add_u32 m0, s73, 16384\n"
  "s_add_u32 s40, s52, 589824\n"
  "s_sub_u32 s41, s40, 1966080\n"
  "s_cselect_b32 s40, s40, s41\n"
  "buffer_load_dwordx4 v104, s[24:27], s40 offen offset:0 lds\n"
  "buffer_load_dwordx4 v105, s[24:27], s40 offen offset:1024 lds\n"
  "buffer_load_dwordx4 v106, s[24:27], s40 offen offset:2048 lds\n"
  "s_sleep 1\n"
  "ds_write_b128 v94, v[48:51] offset:12288\n"
  "s_sleep 1\n"
  "ds_write_b128 v94, v[52:55] offset:13312\n"
  "s_sleep 1\n"
  "ds_write_b128 v94, v[56:59] offset:14336\n"
  "s_sleep 1\n"
  "ds_write_b128 v94, v[60:63] offset:15360\n"
  "s_waitcnt vmcnt(7) lgkmcnt(0)\n"
  "s_barrier\n"
  "s_add_u32 m0, s73, 28672\n"
  "s_add_u32 s40, s52, 786432\n"
  "s_sub_u32 s41, s40, 1966080\n"
  "s_cselect_b32 s40, s40, s41\n"
  "buffer_load_dwordx4 v104, s[24:27], s40 offen offset:0 lds\n"
  "buffer_load_dwordx4 v105, s[24:27], s40 offen offset:1024 lds\n"
  "buffer_load_dwordx4 v106, s[24:27], s40 offen offset:2048 lds\n"
  "s_sleep 1\n"
  "ds_write_b128 v94, v[64:67] offset:16384\n"
  "s_sleep 1\n"
  "ds_write_b128 v94, v[68:71] offset:17408\n"
  "s_sleep 1\n"
  "ds_write_b128 v94, v[72:75] offset:18432\n"
  "s_sleep 1\n"
  "ds_write_b128 v94, v[76:79] offset:19456\n"
  "s_waitcnt vmcnt(6) lgkmcnt(0)\n"
  "s_barrier\n"
  "s_waitcnt vmcnt(3)\n"
  "s_barrier\n"
  "s_setprio 3\n"
  "s_mov_b32 s44, 0\n"
  "buffer_load_dwordx4 v[112:115], v109, s[16:19], 0 offen sc1\n"
  "buffer_load_dwordx4 v[116:119], v109, s[16:19], 0 offen offset:16 sc1\n"
  "s_waitcnt vmcnt(2)\n"
  "s_barrier\n"
  "s_cmp_lg_u32 s44, 0\n"
  "s_cbranch_scc1 19f\n"
  "s_waitcnt vmcnt(0)\n"
  "v_cmp_ne_u64 vcc, s[46:47], v[112:113]\n"
  "v_cmp_ne_u64 s[40:41], s[46:47], v[114:115]\n"
  "v_cmp_ne_u64 s[42:43], s[46:47], v[116:117]\n"
  "s_or_b64 vcc, vcc, s[40:41]\n"
  "v_cmp_ne_u64 s[40:41], s[46:47], v[118:119]\n"
  "s_or_b64 vcc, vcc, s[42:43]\n"
  "s_or_b64 vcc, vcc, s[40:41]\n"
  "s_cbranch_vccnz 29f\n"
  "s_mov_b32 s44, 1\n"
  "s_add_u32 m0, s50, 0\n"
  "s_nop 0\n"
  "buffer_load_dwordx4 v160, s[16:19], s53 offen offset:0 sc1 lds\n"
  "buffer_load_dwordx4 v160, s[16:19], s53 offen offset:1024 sc1 lds\n"
  "buffer_load_dwordx4 v160, s[16:19], s54 offen offset:2048 sc1 lds\n"
  "s_add_u32 m0, s50, 4096\n"
  "s_nop 0\n"
  "buffer_load_dwordx4 v160, s[16:19], s55 offen offset:0 sc1 lds\n"
  "buffer_load_dwordx4 v160, s[16:19], s56 offen offset:1024 sc1 lds\n"
  "s_add_u32 m0, s74, 0\n"
  "s_add_u32 s40, s57, 0\n"
  "s_add_u32 s41, s58, 0\n"
  "buffer_load_dwordx4 v160, s[16:19], s40 offen offset:0 sc1 lds\n"
  "buffer_load_dwordx4 v160, s[16:19], s40 offen offset:1024 sc1 lds\n"
  "buffer_load_dwordx4 v160, s[16:19], s41 offen offset:2048 sc1 lds\n"
  "s_branch 19f\n"
  "29:\n"
  "buffer_load_dwordx4 v[112:115], v109, s[16:19], 0 offen sc1\n"
  "buffer_load_dwordx4 v[116:119], v109, s[16:19], 0 offen offset:16 sc1\n"
  "19:\n"
  "s_barrier\n"
  "v_mov_b32 v80, 0x3f80\n"
  "v_mov_b32 v81, 0\n"
  "v_mov_b32 v82, 135168\n"
  "ds_write_b64 v82, v[80:81] offset:0\n"
  "ds_write_b64 v82, v[80:81] offset:12352\n"
  "ds_write_b64 v82, v[80:81] offset:24704\n"
  "s_cmp_lg_u32 s44, 0\n"
  "s_cbranch_scc1 8f\n"
  "s_mov_b32 s45, 0\n"
  "6:\n"
  "s_waitcnt vmcnt(0)\n"
  "v_cmp_ne_u64 vcc, s[46:47], v[112:113]\n"
  "v_cmp_ne_u64 s[40:41], s[46:47], v[114:115]\n"
  "v_cmp_ne_u64 s[42:43], s[46:47], v[116:117]\n"
  "s_or_b64 vcc, vcc, s[40:41]\n"
  "v_cmp_ne_u64 s[40:41], s[46:47], v[118:119]\n"
  "s_or_b64 vcc, vcc, s[42:43]\n"
  "s_or_b64 vcc, vcc, s[40:41]\n"
  "s_cbranch_vccz 7f\n"
  "s_add_u32 s45, s45, 1\n"
  "s_cmp_gt_u32 s45, 524288\n"
  "s_cbranch_scc1 7f\n"
  "buffer_load_dwordx4 v[112:115], v109, s[16:19], 0 offen sc1\n"
  "buffer_load_dwordx4 v[116:119], v109, s[16:19], 0 offen offset:16 sc1\n"
  "s_branch 6b\n"
  "7:\n"
  "s_add_u32 m0, s50, 0\n"
  "s_nop 0\n"
  "buffer_load_dwordx4 v160, s[16:19], s53 offen offset:0 sc1 lds\n"
  "buffer_load_dwordx4 v160, s[16:19], s53 offen offset:1024 sc1 lds\n"
  "buffer_load_dwordx4 v160, s[16:19], s54 offen offset:2048 sc1 lds\n"
  "s_add_u32 m0, s50, 4096\n"
  "s_nop 0\n"
  "buffer_load_dwordx4 v160, s[16:19], s55 offen offset:0 sc1 lds\n"
  "buffer_load_dwordx4 v160, s[16:19], s56 offen offset:1024 sc1 lds\n"
  "s_add_u32 m0, s74, 0\n"
  "s_add_u32 s40, s57, 0\n"
  "s_add_u32 s41, s58, 0\n"
  "buffer_load_dwordx4 v160, s[16:19], s40 offen offset:0 sc1 lds\n"
  "buffer_load_dwordx4 v160, s[16:19], s40 offen offset:1024 sc1 lds\n"
  "buffer_load_dwordx4 v160, s[16:19], s41 offen offset:2048 sc1 lds\n"
  "8:\n"
  "s_add_u32 m0, s74, 12352\n"
  "s_add_u32 s40, s57, 2048\n"
  "s_add_u32 s41, s58, 1024\n"
  "buffer_load_dwordx4 v160, s[16:19], s40 offen offset:0 sc1 lds\n"
  "buffer_load_dwordx4 v160, s[16:19], s40 offen offset:1024 sc1 lds\n"
  "buffer_load_dwordx4 v160, s[16:19], s41 offen offset:2048 sc1 lds\n"
  "s_waitcnt lgkmcnt(0)\n"
  "s_setprio 0\n"
  "s_waitcnt vmcnt(3)\n"
  "s_barrier\n"
  "s_sleep 4\n"
  "s_add_u32 m0, s74, 24704\n"
  "s_add_u32 s40, s57, 4096\n"
  "s_add_u32 s41, s58, 2048\n"
  "buffer_load_dwordx4 v160, s[16:19], s40 offen offset:0 sc1 lds\n"
  "buffer_load_dwordx4 v160, s[16:19], s40 offen offset:1024 sc1 lds\n"
  "buffer_load_dwordx4 v160, s[16:19], s41 offen offset:2048 sc1 lds\n"
  "s_waitcnt vmcnt(3)\n"
  "s_barrier\n"
  "s_sleep 4\n"
  "s_add_u32 m0, s74, 0\n"
  "s_add_u32 s40, s57, 6144\n"
  "s_add_u32 s41, s58, 3072\n"
  "buffer_load_dwordx4 v160, s[16:19], s40 offen offset:0 sc1 lds\n"
  "buffer_load_dwordx4 v160, s[16:19], s40 offen offset:1024 sc1 lds\n"
  "buffer_load_dwordx4 v160, s[16:19], s41 offen offset:2048 sc1 lds\n"
  "s_waitcnt vmcnt(3)\n"
  "s_barrier\n"
  "s_movk_i32 s40, 0x4000\n"
  "buffer_store_dwordx2 v[110:111], v108, s[16:19], s40 offen sc1\n"
  "buffer_store_dwordx2 v[110:111], v107, s[16:19], 0 offen sc1\n"
  "s_sleep 4\n"
  "s_add_u32 m0, s74, 12352\n"
  "s_add_u32 s40, s57, 8192\n"
  "s_add_u32 s41, s58, 4096\n"
  "buffer_load_dwordx4 v160, s[16:19], s40 offen offset:0 sc1 lds\n"
  "buffer_load_dwordx4 v160, s[16:19], s40 offen offset:1024 sc1 lds\n"
  "buffer_load_dwordx4 v160, s[16:19], s41 offen offset:2048 sc1 lds\n"
  "s_add_u32 m0, s77, 0\n"
  "s_add_u32 s40, s65, 0\n"
  "buffer_load_dwordx4 v162, s[28:31], s40 offen offset:0 lds\n"
  "buffer_load_dwordx4 v163, s[28:31], s40 offen offset:1024 lds\n"
  "buffer_load_dwordx4 v164, s[28:31], s40 offen offset:2048 lds\n"
  "s_waitcnt vmcnt(8)\n"
  "s_barrier\n"
  "s_sleep 4\n"
  "s_add_u32 m0, s74, 24704\n"
  "s_add_u32 s40, s57, 10240\n"
  "s_add_u32 s41, s58, 5120\n"
  "buffer_load_dwordx4 v160, s[16:19], s40 offen offset:0 sc1 lds\n"
  "buffer_load_dwordx4 v160, s[16:19], s40 offen offset:1024 sc1 lds\n"
  "buffer_load_dwordx4 v160, s[16:19], s41 offen offset:2048 sc1 lds\n"
  "s_add_u32 m0, s77, 3072\n"
  "s_add_u32 s40, s65, 20480\n"
  "buffer_load_dwordx4 v162, s[28:31], s40 offen offset:0 lds\n"
  "buffer_load_dwordx4 v163, s[28:31], s40 offen offset:1024 lds\n"
  "buffer_load_dwordx4 v164, s[28:31], s40 offen offset:2048 lds\n"
  "s_waitcnt vmcnt(9)\n"
  "s_barrier\n"
  "s_setprio 3\n"
  "s_movk_i32 s44, 0x4000\n"
  "buffer_load_dwordx4 v[112:115], v109, s[16:19], s44 offen sc1\n"
  "buffer_load_dwordx4 v[116:119], v109, s[16:19], s44 offen offset:16 sc1\n"
  "s_mov_b32 s45, 0\n"
  "6:\n"
  "s_waitcnt vmcnt(0)\n"
  "v_cmp_ne_u64 vcc, s[46:47], v[112:113]\n"
  "v_cmp_ne_u64 s[40:41], s[46:47], v[114:115]\n"
  "v_cmp_ne_u64 s[42:43], s[46:47], v[116:117]\n"
  "s_or_b64 vcc, vcc, s[40:41]\n"
  "v_cmp_ne_u64 s[40:41], s[46:47], v[118:119]\n"
  "s_or_b64 vcc, vcc, s[42:43]\n"
  "s_or_b64 vcc, vcc, s[40:41]\n"
  "s_cbranch_vccz 7f\n"
  "s_add_u32 s45, s45, 1\n"
  "s_cmp_gt_u32 s45, 524288\n"
  "s_cbranch_scc1 7f\n"
  "buffer_load_dwordx4 v[112:115], v109, s[16:19], s44 offen sc1\n"
  "buffer_load_dwordx4 v[116:119], v109, s[16:19], s44 offen offset:16 sc1\n"
  "s_branch 6b\n"
  "7:\n"
  "s_setprio 0\n"
  "s_add_u32 m0, s74, 0\n"
  "s_add_u32 s40, s57, 12288\n"
  "s_add_u32 s41, s58, 6144\n"
  "buffer_load_dwordx4 v160, s[16:19], s40 offen offset:0 sc1 lds\n"
  "buffer_load_dwordx4 v160, s[16:19], s40 offen offset:1024 sc1 lds\n"
  "buffer_load_dwordx4 v160, s[16:19], s41 offen offset:2048 sc1 lds\n"
  "s_add_u32 s41, s60, 0\n"
  "buffer_load_dwordx4 v[80:83], v160, s[16:19], s41 offen sc1\n"
  "s_add_u32 s41, s62, 0\n"
  "buffer_load_dwordx4 v[88:91], v161, s[16:19], s41 offen sc1\n"
  "s_add_u32 s41, s63, 0\n"
  "buffer_load_dwordx4 v[92:95], v161, s[16:19], s41 offen sc1\n"
  "s_add_u32 s41, s64, 0\n"
  "buffer_load_dwordx4 v[96:99], v161, s[16:19], s41 offen sc1\n"
  "s_add_u32 s41, s61, 0\n"
  "buffer_load_dwordx4 v[84:87], v160, s[16:19], s41 offen sc1\n"
  "s_add_u32 s41, s60, 1024\n"
  "buffer_load_dwordx4 v[100:103], v160, s[16:19], s41 offen sc1\n"
  "s_add_u32 s41, s62, 20992\n"
  "buffer_load_dwordx4 v[108:111], v161, s[16:19], s41 offen sc1\n"
  "s_add_u32 s41, s63, 20992\n"
  "buffer_load_dwordx4 v[112:115], v161, s[16:19], s41 offen sc1\n"
  "s_add_u32 s41, s64, 20992\n"
  "buffer_load_dwordx4 v[116:119], v161, s[16:19], s41 offen sc1\n"
  "s_add_u32 s41, s61, 1024\n"
  "buffer_load_dwordx4 v[104:107], v160, s[16:19], s41 offen sc1\n"
  "s_add_u32 s41, s60, 2048\n"
  "buffer_load_dwordx4 v[120:123], v160, s[16:19], s41 offen sc1\n"
  "s_add_u32 s41, s62, 41984\n"
  "buffer_load_dwordx4 v[128:131], v161, s[16:19], s41 offen sc1\n"
  "s_add_u32 s41, s63, 41984\n"
  "buffer_load_dwordx4 v[132:135], v161, s[16:19], s41 offen sc1\n"
  "s_add_u32 s41, s64, 41984\n"
  "buffer_load_dwordx4 v[136:139], v161, s[16:19], s41 offen sc1\n"
  "s_add_u32 s41, s61, 2048\n"
  "buffer_load_dwordx4 v[124:127], v160, s[16:19], s41 offen sc1\n"
  "s_barrier\n"
  "s_waitcnt vmcnt(10)\n"
  "v_mfma_f32_32x32x16_bf16 v[0:15], v[88:91], v[80:83], 0\n"
  "s_add_u32 s41, s60, 3072\n"
  "buffer_load_dwordx4 v[140:143], v160, s[16:19], s41 offen sc1\n"
  "v_mfma_f32_32x32x16_bf16 v[16:31], v[92:95], v[80:83], 0\n"
  "s_add_u32 s41, s62, 62976\n"
  "buffer_load_dwordx4 v[148:151], v161, s[16:19], s41 offen sc1\n"
  "v_mfma_f32_32x32x16_bf16 v[32:47], v[96:99], v[80:83], 0\n"
  "s_add_u32 s41, s63, 62976\n"
  "buffer_load_dwordx4 v[152:155], v161, s[16:19], s41 offen sc1\n"
  "s_add_u32 m0, s74, 12352\n"
  "s_add_u32 s40, s57, 14336\n"
  "s_add_u32 s41, s58, 7168\n"
  "buffer_load_dwordx4 v160, s[16:19], s40 offen offset:0 sc1 lds\n"
  "buffer_load_dwordx4 v160, s[16:19], s40 offen offset:1024 sc1 lds\n"
  "buffer_load_dwordx4 v160, s[16:19], s41 offen offset:2048 sc1 lds\n"
  "s_add_u32 m0, s77, 6144\n"
  "s_add_u32 s40, s65, 40960\n"
  "buffer_load_dwordx4 v162, s[28:31], s40 offen offset:0 lds\n"
  "buffer_load_dwordx4 v163, s[28:31], s40 offen offset:1024 lds\n"
  "buffer_load_dwordx4 v164, s[28:31], s40 offen offset:2048 lds\n"
  "v_mfma_f32_32x32x16_bf16 v[48:63], v[88:91], v[84:87], 0\n"
  "s_add_u32 s41, s64, 62976\n"
  "buffer_load_dwordx4 v[156:159], v161, s[16:19], s41 offen sc1\n"
  "v_mfma_f32_32x32x16_bf16 v[64:79], v[92:95], v[84:87], 0\n"
  "s_add_u32 s41, s61, 3072\n"
  "buffer_load_dwordx4 v[144:147], v160, s[16:19], s41 offen sc1\n"
  "s_waitcnt vmcnt(16)\n"
  "v_mfma_f32_32x32x16_bf16 v[0:15], v[108:111], v[100:103], v[0:15]\n"
  "s_add_u32 s41, s60, 4096\n"
  "buffer_load_dwordx4 v[80:83], v160, s[16:19], s41 offen sc1\n"
  "v_mfma_f32_32x32x16_bf16 v[16:31], v[112:115], v[100:103], v[16:31]\n"
  "s_add_u32 s41, s62, 83968\n"
  "buffer_load_dwordx4 v[88:91], v161, s[16:19], s41 offen sc1\n"
  "v_mfma_f32_32x32x16_bf16 v[32:47], v[116:119], v[100:103], v[32:47]\n"
  "s_add_u32 s41, s63, 83968\n"
  "buffer_load_dwordx4 v[92:95], v161, s[16:19], s41 offen sc1\n"
  "v_mfma_f32_32x32x16_bf16 v[48:63], v[108:111], v[104:107], v[48:63]\n"
  "s_add_u32 s41, s64, 83968\n"
  "buffer_load_dwordx4 v[96:99], v161, s[16:19], s41 offen sc1\n"
  "s_barrier\n"
  "v_mfma_f32_32x32x16_bf16 v[64:79], v[112:115], v[104:107], v[64:79]\n"
  "s_add_u32 s41, s61, 4096\n"
  "buffer_load_dwordx4 v[84:87], v160, s[16:19], s41 offen sc1\n"
  "s_waitcnt vmcnt(16)\n"
  "v_mfma_f32_32x32x16_bf16 v[0:15], v[128:131], v[120:123], v[0:15]\n"
  "s_add_u32 s41, s60, 5120\n"
  "buffer_load_dwordx4 v[100:103], v160, s[16:19], s41 offen sc1\n"
  "v_mfma_f32_32x32x16_bf16 v[16:31], v[132:135], v[120:123], v[16:31]\n"
  "s_add_u32 s41, s62, 104960\n"
  "buffer_load_dwordx4 v[108:111], v161, s[16:19], s41 offen sc1\n"
  "s_add_u32 m0, s74, 24704\n"
  "s_add_u32 s40, s57, 16384\n"
  "s_add_u32 s41, s58, 8192\n"
  "buffer_load_dwordx4 v160, s[16:19], s40 offen offset:0 sc1 lds\n"
  "buffer_load_dwordx4 v160, s[16:19], s40 offen offset:1024 sc1 lds\n"
  "buffer_load_dwordx4 v160, s[16:19], s41 offen offset:2048 sc1 lds\n"
  "s_add_u32 m0, s77, 9216\n"
  "s_add_u32 s40, s65, 61440\n"
  "buffer_load_dwordx4 v162, s[28:31], s40 offen offset:0 lds\n"
  "buffer_load_dwordx4 v163, s[28:31], s40 offen offset:1024 lds\n"
  "buffer_load_dwordx4 v164, s[28:31], s40 offen offset:2048 lds\n"
  "v_mfma_f32_32x32x16_bf16 v[32:47], v[136:139], v[120:123], v[32:47]\n"
  "s_add_u32 s41, s63, 104960\n"
  "buffer_load_dwordx4 v[112:115], v161, s[16:19], s41 offen sc1\n"
  "v_mfma_f32_32x32x16_bf16 v[48:63], v[128:131], v[124:127], v[48:63]\n"
  "s_add_u32 s41, s64, 104960\n"
  "buffer_load_dwordx4 v[116:119], v161, s[16:19], s41 offen sc1\n"
  "v_mfma_f32_32x32x16_bf16 v[64:79], v[132:135], v[124:127], v[64:79]\n"
  "s_add_u32 s41, s61, 5120\n"
  "buffer_load_dwordx4 v[104:107], v160, s[16:19], s41 offen sc1\n"
  "s_waitcnt vmcnt(16)\n"
  "v_mfma_f32_32x32x16_bf16 v[0:15], v[148:151], v[140:143], v[0:15]\n"
  "s_add_u32 s41, s60, 6144\n"
  "buffer_load_dwordx4 v[120:123], v160, s[16:19], s41 offen sc1\n"
  "v_mfma_f32_32x32x16_bf16 v[16:31], v[152:155], v[140:143], v[16:31]\n"
  "s_add_u32 s41, s62, 125952\n"
  "buffer_load_dwordx4 v[128:131], v161, s[16:19], s41 offen sc1\n"
  "v_mfma_f32_32x32x16_bf16 v[32:47], v[156:159], v[140:143], v[32:47]\n"
  "s_add_u32 s41, s63, 125952\n"
  "buffer_load_dwordx4 v[132:135], v161, s[16:19], s41 offen sc1\n"
  "s_barrier\n"
  "v_mfma_f32_32x32x16_bf16 v[48:63], v[148:151], v[144:147], v[48:63]\n"
  "s_add_u32 s41, s64, 125952\n"
  "buffer_load_dwordx4 v[136:139], v161, s[16:19], s41 offen sc1\n"
  "v_mfma_f32_32x32x16_bf16 v[64:79], v[152:155], v[144:147], v[64:79]\n"
  "s_add_u32 s41, s61, 6144\n"
  "buffer_load_dwordx4 v[124:127], v160, s[16:19], s41 offen sc1\n"
  "s_waitcnt vmcnt(16)\n"
  "v_mfma_f32_32x32x16_bf16 v[0:15], v[88:91], v[80:83], v[0:15]\n"
  "s_add_u32 s41, s60, 7168\n"
  "buffer_load_dwordx4 v[140:143], v160, s[16:19], s41 offen sc1\n"
  "s_add_u32 m0, s74, 0\n"
  "s_add_u32 s40, s57, 18432\n"
  "s_add_u32 s41, s58, 9216\n"
  "buffer_load_dwordx4 v160, s[16:19], s40 offen offset:0 sc1 lds\n"
  "buffer_load_dwordx4 v160, s[16:19], s40 offen offset:1024 sc1 lds\n"
  "buffer_load_dwordx4 v160, s[16:19], s41 offen offset:2048 sc1 lds\n"
  "s_add_u32 m0, s77, 12288\n"
  "s_add_u32 s40, s65, 81920\n"
  "buffer_load_dwordx4 v162, s[28:31], s40 offen offset:0 lds\n"
  "buffer_load_dwordx4 v163, s[28:31], s40 offen offset:1024 lds\n"
  "buffer_load_dwordx4 v164, s[28:31], s40 offen offset:2048 lds\n"
  "v_mfma_f32_32x32x16_bf16 v[16:31], v[92:95], v[80:83], v[16:31]\n"
  "s_add_u32 s41, s62, 146944\n"
  "buffer_load_dwordx4 v[148:151], v161, s[16:19], s41 offen sc1\n"
  "v_mfma_f32_32x32x16_bf16 v[32:47], v[96:99], v[80:83], v[32:47]\n"
  "s_add_u32 s41, s63, 146944\n"
  "buffer_load_dwordx4 v[152:155], v161, s[16:19], s41 offen sc1\n"
  "v_mfma_f32_32x32x16_bf16 v[48:63], v[88:91], v[84:87], v[48:63]\n"
  "s_add_u32 s41, s64, 146944\n"
  "buffer_load_dwordx4 v[156:159], v161, s[16:19], s41 offen sc1\n"
  "v_mfma_f32_32x32x16_bf16 v[64:79], v[92:95], v[84:87], v[64:79]\n"
  "s_add_u32 s41, s61, 7168\n"
  "buffer_load_dwordx4 v[144:147], v160, s[16:19], s41 offen sc1\n"
  "s_waitcnt vmcnt(16)\n"
  "v_mfma_f32_32x32x16_bf16 v[0:15], v[108:111], v[100:103], v[0:15]\n"
  "s_add_u32 s41, s60, 8192\n"
  "buffer_load_dwordx4 v[80:83], v160, s[16:19], s41 offen sc1\n"
  "v_mfma_f32_32x32x16_bf16 v[16:31], v[112:115], v[100:103], v[16:31]\n"
  "s_add_u32 s41, s62, 167936\n"
  "buffer_load_dwordx4 v[88:91], v161, s[16:19], s41 offen sc1\n"
  "s_barrier\n"
  "v_mfma_f32_32x32x16_bf16 v[32:47], v[116:119], v[100:103], v[32:47]\n"
  "s_add_u32 s41, s63, 167936\n"
  "buffer_load_dwordx4 v[92:95], v161, s[16:19], s41 offen sc1\n"
  "v_mfma_f32_32x32x16_bf16 v[48:63], v[108:111], v[104:107], v[48:63]\n"
  "s_add_u32 s41, s64, 167936\n"
  "buffer_load_dwordx4 v[96:99], v161, s[16:19], s41 offen sc1\n"
  "v_mfma_f32_32x32x16_bf16 v[64:79], v[112:115], v[104:107], v[64:79]\n"
  "s_add_u32 s41, s61, 8192\n"
  "buffer_load_dwordx4 v[84:87], v160, s[16:19], s41 offen sc1\n"
  "s_add_u32 m0, s74, 12352\n"
  "s_add_u32 s40, s57, 20480\n"
  "s_add_u32 s41, s58, 10240\n"
  "buffer_load_dwordx4 v160, s[16:19], s40 offen offset:0 sc1 lds\n"
  "buffer_load_dwordx4 v160, s[16:19], s40 offen offset:1024 sc1 lds\n"
  "buffer_load_dwordx4 v160, s[16:19], s41 offen offset:2048 sc1 lds\n"
  "s_add_u32 m0, s77, 15360\n"
  "s_add_u32 s40, s65, 102400\n"
  "buffer_load_dwordx4 v162, s[28:31], s40 offen offset:0 lds\n"
  "buffer_load_dwordx4 v163, s[28:31], s40 offen offset:1024 lds\n"
  "buffer_load_dwordx4 v164, s[28:31], s40 offen offset:2048 lds\n"
  "s_waitcnt vmcnt(22)\n"
  "v_mfma_f32_32x32x16_bf16 v[0:15], v[128:131], v[120:123], v[0:15]\n"
  "s_add_u32 s41, s60, 9216\n"
  "buffer_load_dwordx4 v[100:103], v160, s[16:19], s41 offen sc1\n"
  "v_mfma_f32_32x32x16_bf16 v[16:31], v[132:135], v[120:123], v[16:31]\n"
  "s_add_u32 s41, s62, 188928\n"
  "buffer_load_dwordx4 v[108:111], v161, s[16:19], s41 offen sc1\n"
  "v_mfma_f32_32x32x16_bf16 v[32:47], v[136:139], v[120:123], v[32:47]\n"
  "s_add_u32 s41, s63, 188928\n"
  "buffer_load_dwordx4 v[112:115], v161, s[16:19], s41 offen sc1\n"
  "v_mfma_f32_32x32x16_bf16 v[48:63], v[128:131], v[124:127], v[48:63]\n"
  "s_add_u32 s41, s64, 188928\n"
  "buffer_load_dwordx4 v[116:119], v161, s[16:19], s41 offen sc1\n"
  "v_mfma_f32_32x32x16_bf16 v[64:79], v[132:135], v[124:127], v[64:79]\n"
  "s_add_u32 s41, s61, 9216\n"
  "buffer_load_dwordx4 v[104:107], v160, s[16:19], s41 offen sc1\n"
  "s_waitcnt vmcnt(16)\n"
  "v_mfma_f32_32x32x16_bf16 v[0:15], v[148:151], v[140:143], v[0:15]\n"
  "s_add_u32 s41, s60, 10240\n"
  "buffer_load_dwordx4 v[120:123], v160, s[16:19], s41 offen sc1\n"
  "s_barrier\n"
  "v_mfma_f32_32x32x16_bf16 v[16:31], v[152:155], v[140:143], v[16:31]\n"
  "s_add_u32 s41, s62, 209920\n"
  "buffer_load_dwordx4 v[128:131], v161, s[16:19], s41 offen sc1\n"
  "v_mfma_f32_32x32x16_bf16 v[32:47], v[156:159], v[140:143], v[32:47]\n"
  "s_add_u32 s41, s63, 209920\n"
  "buffer_load_dwordx4 v[132:135], v161, s[16:19], s41 offen sc1\n"
  "v_mfma_f32_32x32x16_bf16 v[48:63], v[148:151], v[144:147], v[48:63]\n"
  "s_add_u32 s41, s64, 209920\n"
  "buffer_load_dwordx4 v[136:139], v161, s[16:19], s41 offen sc1\n"
  "s_add_u32 m0, s74, 24704\n"
  "s_add_u32 s40, s57, 22528\n"
  "s_add_u32 s41, s58, 11264\n"
  "buffer_load_dwordx4 v160, s[16:19], s40 offen offset:0 sc1 lds\n"
  "buffer_load_dwordx4 v160, s[16:19], s40 offen offset:1024 sc1 lds\n"
  "buffer_load_dwordx4 v160, s[16:19], s41 offen offset:2048 sc1 lds\n"
  "s_add_u32 m0, s77, 18432\n"
  "s_add_u32 s40, s65, 122880\n"
  "buffer_load_dwordx4 v162, s[28:31], s40 offen offset:0 lds\n"
  "buffer_load_dwordx4 v163, s[28:31], s40 offen offset:1024 lds\n"
  "buffer_load_dwordx4 v164, s[28:31], s40 offen offset:2048 lds\n"
  "v_mfma_f32_32x32x16_bf16 v[64:79], v[152:155], v[144:147], v[64:79]\n"
  "s_add_u32 s41, s61, 10240\n"
  "buffer_load_dwordx4 v[124:127], v160, s[16:19], s41 offen sc1\n"
  "s_waitcnt vmcnt(22)\n"
  "v_mfma_f32_32x32x16_bf16 v[0:15], v[88:91], v[80:83], v[0:15]\n"
  "s_add_u32 s41, s60, 11264\n"
  "buffer_load_dwordx4 v[140:143], v160, s[16:19], s41 offen sc1\n"
  "v_mfma_f32_32x32x16_bf16 v[16:31], v[92:95], v[80:83], v[16:31]\n"
  "s_add_u32 s41, s62, 230912\n"
  "buffer_load_dwordx4 v[148:151], v161, s[16:19], s41 offen sc1\n"
  "v_mfma_f32_32x32x16_bf16 v[32:47], v[96:99], v[80:83], v[32:47]\n"
  "s_add_u32 s41, s63, 230912\n"
  "buffer_load_dwordx4 v[152:155], v161, s[16:19], s41 offen sc1\n"
  "v_mfma_f32_32x32x16_bf16 v[48:63], v[88:91], v[84:87], v[48:63]\n"
  "s_add_u32 s41, s64, 230912\n"
  "buffer_load_dwordx4 v[156:159], v161, s[16:19], s41 offen sc1\n"
  "v_mfma_f32_32x32x16_bf16 v[64:79], v[92:95], v[84:87], v[64:79]\n"
  "s_add_u32 s41, s61, 11264\n"
  "buffer_load_dwordx4 v[144:147], v160, s[16:19], s41 offen sc1\n"
  "s_waitcnt vmcnt(24)\n"
  "s_barrier\n"
  "s_waitcnt vmcnt(16)\n"
  "v_mfma_f32_32x32x16_bf16 v[0:15], v[108:111], v[100:103], v[0:15]\n"
  "s_add_u32 s41, s60, 12288\n"
  "buffer_load_dwordx4 v[80:83], v160, s[16:19], s41 offen sc1\n"
  "v_mfma_f32_32x32x16_bf16 v[16:31], v[112:115], v[100:103], v[16:31]\n"
  "s_add_u32 s41, s62, 251904\n"
  "buffer_load_dwordx4 v[88:91], v161, s[16:19], s41 offen sc1\n"
  "v_mfma_f32_32x32x16_bf16 v[32:47], v[116:119], v[100:103], v[32:47]\n"
  "s_add_u32 s41, s63, 251904\n"
  "buffer_load_dwordx4 v[92:95], v161, s[16:19], s41 offen sc1\n"
  "s_add_u32 m0, s74, 0\n"
  "s_add_u32 s40, s57, 24576\n"
  "s_add_u32 s41, s58, 12288\n"
  "buffer_load_dwordx4 v160, s[16:19], s40 offen offset:0 sc1 lds\n"
  "buffer_load_dwordx4 v160, s[16:19], s40 offen offset:1024 sc1 lds\n"
  "buffer_load_dwordx4 v160, s[16:19], s41 offen offset:2048 sc1 lds\n"
  "s_add_u32 m0, s77, 21504\n"
  "s_add_u32 s40, s65, 143360\n"
  "buffer_load_dwordx4 v162, s[28:31], s40 offen offset:0 lds\n"
  "buffer_load_dwordx4 v163, s[28:31], s40 offen offset:1024 lds\n"
  "buffer_load_dwordx4 v164, s[28:31], s40 offen offset:2048 lds\n"
  "v_mfma_f32_32x32x16_bf16 v[48:63], v[108:111], v[104:107], v[48:63]\n"
  "s_add_u32 s41, s64, 251904\n"
  "buffer_load_dwordx4 v[96:99], v161, s[16:19], s41 offen sc1\n"
  "v_mfma_f32_32x32x16_bf16 v[64:79], v[112:115], v[104:107], v[64:79]\n"
  "s_add_u32 s41, s61, 12288\n"
  "buffer_load_dwordx4 v[84:87], v160, s[16:19], s41 offen sc1\n"
  "s_waitcnt vmcnt(16)\n"
  "v_mfma_f32_32x32x16_bf16 v[0:15], v[128:131], v[120:123], v[0:15]\n"
  "s_add_u32 s41, s60, 13312\n"
  "buffer_load_dwordx4 v[100:103], v160, s[16:19], s41 offen sc1\n"
  "v_mfma_f32_32x32x16_bf16 v[16:31], v[132:135], v[120:123], v[16:31]\n"
  "s_add_u32 s41, s62, 272896\n"
  "buffer_load_dwordx4 v[108:111], v161, s[16:19], s41 offen sc1\n"
  "v_mfma_f32_32x32x16_bf16 v[32:47], v[136:139], v[120:123], v[32:47]\n"
  "s_add_u32 s41, s63, 272896\n"
  "buffer_load_dwordx4 v[112:115], v161, s[16:19], s41 offen sc1\n"
  "v_mfma_f32_32x32x16_bf16 v[48:63], v[128:131], v[124:127], v[48:63]\n"
  "s_add_u32 s41, s64, 272896\n"
  "buffer_load_dwordx4 v[116:119], v161, s[16:19], s41 offen sc1\n"
  "s_barrier\n"
  "v_mfma_f32_32x32x16_bf16 v[64:79], v[132:135], v[124:127], v[64:79]\n"
  "s_add_u32 s41, s61, 13312\n"
  "buffer_load_dwordx4 v[104:107], v160, s[16:19], s41 offen sc1\n"
  "s_waitcnt vmcnt(16)\n"
  "v_mfma_f32_32x32x16_bf16 v[0:15], v[148:151], v[140:143], v[0:15]\n"
  "s_add_u32 s41, s60, 14336\n"
  "buffer_load_dwordx4 v[120:123], v160, s[16:19], s41 offen sc1\n"
  "v_mfma_f32_32x32x16_bf16 v[16:31], v[152:155], v[140:143], v[16:31]\n"
  "s_add_u32 s41, s62, 293888\n"
  "buffer_load_dwordx4 v[128:131], v161, s[16:19], s41 offen sc1\n"
  "s_add_u32 m0, s74, 12352\n"
  "s_add_u32 s40, s57, 26624\n"
  "s_add_u32 s41, s58, 13312\n"
  "buffer_load_dwordx4 v160, s[16:19], s40 offen offset:0 sc1 lds\n"
  "buffer_load_dwordx4 v160, s[16:19], s40 offen offset:1024 sc1 lds\n"
  "buffer_load_dwordx4 v160, s[16:19], s41 offen offset:2048 sc1 lds\n"
  "s_add_u32 m0, s77, 24576\n"
  "s_add_u32 s40, s65, 163840\n"
  "buffer_load_dwordx4 v162, s[28:31], s40 offen offset:0 lds\n"
  "buffer_load_dwordx4 v163, s[28:31], s40 offen offset:1024 lds\n"
  "buffer_load_dwordx4 v164, s[28:31], s40 offen offset:2048 lds\n"
  "v_mfma_f32_32x32x16_bf16 v[32:47], v[156:159], v[140:143], v[32:47]\n"
  "s_add_u32 s41, s63, 293888\n"
  "buffer_load_dwordx4 v[132:135], v161, s[16:19], s41 offen sc1\n"
  "v_mfma_f32_32x32x16_bf16 v[48:63], v[148:151], v[144:147], v[48:63]\n"
  "s_add_u32 s41, s64, 293888\n"
  "buffer_load_dwordx4 v[136:139], v161, s[16:19], s41 offen sc1\n"
  "v_mfma_f32_32x32x16_bf16 v[64:79], v[152:155], v[144:147], v[64:79]\n"
  "s_add_u32 s41, s61, 14336\n"
  "buffer_load_dwordx4 v[124:127], v160, s[16:19], s41 offen sc1\n"
  "s_waitcnt vmcnt(16)\n"
  "v_mfma_f32_32x32x16_bf16 v[0:15], v[88:91], v[80:83], v[0:15]\n"
  "s_add_u32 s41, s60, 15360\n"
  "buffer_load_dwordx4 v[140:143], v160, s[16:19], s41 offen sc1\n"
  "v_mfma_f32_32x32x16_bf16 v[16:31], v[92:95], v[80:83], v[16:31]\n"
  "s_add_u32 s41, s62, 314880\n"
  "buffer_load_dwordx4 v[148:151], v161, s[16:19], s41 offen sc1\n"
  "v_mfma_f32_32x32x16_bf16 v[32:47], v[96:99], v[80:83], v[32:47]\n"
  "s_add_u32 s41, s63, 314880\n"
  "buffer_load_dwordx4 v[152:155], v161, s[16:19], s41 offen sc1\n"
  "s_barrier\n"
  "v_mfma_f32_32x32x16_bf16 v[48:63], v[88:91], v[84:87], v[48:63]\n"
  "s_add_u32 s41, s64, 314880\n"
  "buffer_load_dwordx4 v[156:159], v161, s[16:19], s41 offen sc1\n"
  "v_mfma_f32_32x32x16_bf16 v[64:79], v[92:95], v[84:87], v[64:79]\n"
  "s_add_u32 s41, s61, 15360\n"
  "buffer_load_dwordx4 v[144:147], v160, s[16:19], s41 offen sc1\n"
  "s_waitcnt vmcnt(16)\n"
  "v_mfma_f32_32x32x16_bf16 v[0:15], v[108:111], v[100:103], v[0:15]\n"
  "s_add_u32 s41, s60, 16384\n"
  "buffer_load_dwordx4 v[80:83], v160, s[16:19], s41 offen sc1\n"
  "s_add_u32 m0, s74, 24704\n"
  "s_add_u32 s40, s57, 28672\n"
  "s_add_u32 s41, s58, 14336\n"
  "buffer_load_dwordx4 v160, s[16:19], s40 offen offset:0 sc1 lds\n"
  "buffer_load_dwordx4 v160, s[16:19], s40 offen offset:1024 sc1 lds\n"
  "buffer_load_dwordx4 v160, s[16:19], s41 offen offset:2048 sc1 lds\n"
  "s_add_u32 m0, s77, 27648\n"
  "s_add_u32 s40, s65, 184320\n"
  "buffer_load_dwordx4 v162, s[28:31], s40 offen offset:0 lds\n"
  "buffer_load_dwordx4 v163, s[28:31], s40 offen offset:1024 lds\n"
  "buffer_load_dwordx4 v164, s[28:31], s40 offen offset:2048 lds\n"
  "v_mfma_f32_32x32x16_bf16 v[16:31], v[112:115], v[100:103], v[16:31]\n"
  "s_add_u32 s41, s62, 335872\n"
  "buffer_load_dwordx4 v[88:91], v161, s[16:19], s41 offen sc1\n"
  "v_mfma_f32_32x32x16_bf16 v[32:47], v[116:119], v[100:103], v[32:47]\n"
  "s_add_u32 s41, s63, 335872\n"
  "buffer_load_dwordx4 v[92:95], v161, s[16:19], s41 offen sc1\n"
  "v_mfma_f32_32x32x16_bf16 v[48:63], v[108:111], v[104:107], v[48:63]\n"
  "s_add_u32 s41, s64, 335872\n"
  "buffer_load_dwordx4 v[96:99], v161, s[16:19], s41 offen sc1\n"
  "v_mfma_f32_32x32x16_bf16 v[64:79], v[112:115], v[104:107], v[64:79]\n"
  "s_add_u32 s41, s61, 16384\n"
  "buffer_load_dwordx4 v[84:87], v160, s[16:19], s41 offen sc1\n"
  "s_waitcnt vmcnt(16)\n"
  "v_mfma_f32_32x32x16_bf16 v[0:15], v[128:131], v[120:123], v[0:15]\n"
  "s_add_u32 s41, s60, 17408\n"
  "buffer_load_dwordx4 v[100:103], v160, s[16:19], s41 offen sc1\n"
  "v_mfma_f32_32x32x16_bf16 v[16:31], v[132:135], v[120:123], v[16:31]\n"
  "s_add_u32 s41, s62, 356864\n"
  "buffer_load_dwordx4 v[108:111], v161, s[16:19], s41 offen sc1\n"
  "s_barrier\n"
  "v_mfma_f32_32x32x16_bf16 v[32:47], v[136:139], v[120:123], v[32:47]\n"
  "s_add_u32 s41, s63, 356864\n"
  "buffer_load_dwordx4 v[112:115], v161, s[16:19], s41 offen sc1\n"
  "v_mfma_f32_32x32x16_bf16 v[48:63], v[128:131], v[124:127], v[48:63]\n"
  "s_add_u32 s41, s64, 356864\n"
  "buffer_load_dwordx4 v[116:119], v161, s[16:19], s41 offen sc1\n"
  "v_mfma_f32_32x32x16_bf16 v[64:79], v[132:135], v[124:127], v[64:79]\n"
  "s_add_u32 s41, s61, 17408\n"
  "buffer_load_dwordx4 v[104:107], v160, s[16:19], s41 offen sc1\n"
  "s_add_u32 m0, s74, 0\n"
  "s_add_u32 s40, s57, 30720\n"
  "s_add_u32 s41, s58, 15360\n"
  "buffer_load_dwordx4 v160, s[16:19], s40 offen offset:0 sc1 lds\n"
  "buffer_load_dwordx4 v160, s[16:19], s40 offen offset:1024 sc1 lds\n"
  "buffer_load_dwordx4 v160, s[16:19], s41 offen offset:2048 sc1 lds\n"
  "s_waitcnt vmcnt(19)\n"
  "v_mfma_f32_32x32x16_bf16 v[0:15], v[148:151], v[140:143], v[0:15]\n"
  "s_add_u32 s41, s60, 18432\n"
  "buffer_load_dwordx4 v[120:123], v160, s[16:19], s41 offen sc1\n"
  "v_mfma_f32_32x32x16_bf16 v[16:31], v[152:155], v[140:143], v[16:31]\n"
  "s_add_u32 s41, s62, 377856\n"
  "buffer_load_dwordx4 v[128:131], v161, s[16:19], s41 offen sc1\n"
  "v_mfma_f32_32x32x16_bf16 v[32:47], v[156:159], v[140:143], v[32:47]\n"
  "s_add_u32 s41, s63, 377856\n"
  "buffer_load_dwordx4 v[132:135], v161, s[16:19], s41 offen sc1\n"
  "v_mfma_f32_32x32x16_bf16 v[48:63], v[148:151], v[144:147], v[48:63]\n"
  "s_add_u32 s41, s64, 377856\n"
  "buffer_load_dwordx4 v[136:139], v161, s[16:19], s41 offen sc1\n"
  "v_mfma_f32_32x32x16_bf16 v[64:79], v[152:155], v[144:147], v[64:79]\n"
  "s_add_u32 s41, s61, 18432\n"
  "buffer_load_dwordx4 v[124:127], v160, s[16:19], s41 offen sc1\n"
  "s_waitcnt vmcnt(13)\n"
  "v_mfma_f32_32x32x16_bf16 v[0:15], v[88:91], v[80:83], v[0:15]\n"
  "s_add_u32 s41, s60, 19456\n"
  "buffer_load_dwordx4 v[140:143], v160, s[16:19], s41 offen sc1\n"
  "s_barrier\n"
  "v_mfma_f32_32x32x16_bf16 v[16:31], v[92:95], v[80:83], v[16:31]\n"
  "s_add_u32 s41, s62, 398848\n"
  "buffer_load_dwordx4 v[148:151], v161, s[16:19], s41 offen sc1\n"
  "v_mfma_f32_32x32x16_bf16 v[32:47], v[96:99], v[80:83], v[32:47]\n"
  "s_add_u32 s41, s63, 398848\n"
  "buffer_load_dwordx4 v[152:155], v161, s[16:19], s41 offen sc1\n"
  "v_mfma_f32_32x32x16_bf16 v[48:63], v[88:91], v[84:87], v[48:63]\n"
  "s_add_u32 s41, s64, 398848\n"
  "buffer_load_dwordx4 v[156:159], v161, s[16:19], s41 offen sc1\n"
  "v_mfma_f32_32x32x16_bf16 v[64:79], v[92:95], v[84:87], v[64:79]\n"
  "s_add_u32 s41, s61, 19456\n"
  "buffer_load_dwordx4 v[144:147], v160, s[16:19], s41 offen sc1\n"
  "s_waitcnt vmcnt(13)\n"
  "v_mfma_f32_32x32x16_bf16 v[0:15], v[108:111], v[100:103], v[0:15]\n"
  "s_add_u32 s41, s60, 20480\n"
  "buffer_load_dwordx4 v[80:83], v160, s[16:19], s41 offen sc1\n"
  "v_mfma_f32_32x32x16_bf16 v[16:31], v[112:115], v[100:103], v[16:31]\n"
  "s_add_u32 s41, s62, 419840\n"
  "buffer_load_dwordx4 v[88:91], v161, s[16:19], s41 offen sc1\n"
  "v_mfma_f32_32x32x16_bf16 v[32:47], v[116:119], v[100:103], v[32:47]\n"
  "s_add_u32 s41, s63, 419840\n"
  "buffer_load_dwordx4 v[92:95], v161, s[16:19], s41 offen sc1\n"
  "v_mfma_f32_32x32x16_bf16 v[48:63], v[108:111], v[104:107], v[48:63]\n"
  "s_add_u32 s41, s64, 419840\n"
  "buffer_load_dwordx4 v[96:99], v161, s[16:19], s41 offen sc1\n"
  "v_mfma_f32_32x32x16_bf16 v[64:79], v[112:115], v[104:107], v[64:79]\n"
  "s_add_u32 s41, s61, 20480\n"
  "buffer_load_dwordx4 v[84:87], v160, s[16:19], s41 offen sc1\n"
  "s_waitcnt vmcnt(15)\n"
  "s_barrier\n"
  "s_waitcnt vmcnt(10)\n"
  "v_mfma_f32_32x32x16_bf16 v[0:15], v[128:131], v[120:123], v[0:15]\n"
  "s_add_u32 s41, s60, 21504\n"
  "buffer_load_dwordx4 v[100:103], v160, s[16:19], s41 offen sc1\n"
  "v_mfma_f32_32x32x16_bf16 v[16:31], v[132:135], v[120:123], v[16:31]\n"
  "s_add_u32 s41, s62, 440832\n"
  "buffer_load_dwordx4 v[108:111], v161, s[16:19], s41 offen sc1\n"
  "v_mfma_f32_32x32x16_bf16 v[32:47], v[136:139], v[120:123], v[32:47]\n"
  "s_add_u32 s41, s63, 440832\n"
  "buffer_load_dwordx4 v[112:115], v161, s[16:19], s41 offen sc1\n"
  "v_mfma_f32_32x32x16_bf16 v[48:63], v[128:131], v[124:127], v[48:63]\n"
  "s_add_u32 s41, s64, 440832\n"
  "buffer_load_dwordx4 v[116:119], v161, s[16:19], s41 offen sc1\n"
  "v_mfma_f32_32x32x16_bf16 v[64:79], v[132:135], v[124:127], v[64:79]\n"
  "s_add_u32 s41, s61, 21504\n"
  "buffer_load_dwordx4 v[104:107], v160, s[16:19], s41 offen sc1\n"
  "s_waitcnt vmcnt(10)\n"
  "v_mfma_f32_32x32x16_bf16 v[0:15], v[148:151], v[140:143], v[0:15]\n"
  "s_add_u32 s41, s60, 22528\n"
  "buffer_load_dwordx4 v[120:123], v160, s[16:19], s41 offen sc1\n"
  "v_mfma_f32_32x32x16_bf16 v[16:31], v[152:155], v[140:143], v[16:31]\n"
  "s_add_u32 s41, s62, 461824\n"
  "buffer_load_dwordx4 v[128:131], v161, s[16:19], s41 offen sc1\n"
  "v_mfma_f32_32x32x16_bf16 v[32:47], v[156:159], v[140:143], v[32:47]\n"
  "s_add_u32 s41, s63, 461824\n"
  "buffer_load_dwordx4 v[132:135], v161, s[16:19], s41 offen sc1\n"
  "v_mfma_f32_32x32x16_bf16 v[48:63], v[148:151], v[144:147], v[48:63]\n"
  "s_add_u32 s41, s64, 461824\n"
  "buffer_load_dwordx4 v[136:139], v161, s[16:19], s41 offen sc1\n"
  "v_mfma_f32_32x32x16_bf16 v[64:79], v[152:155], v[144:147], v[64:79]\n"
  "s_add_u32 s41, s61, 22528\n"
  "buffer_load_dwordx4 v[124:127], v160, s[16:19], s41 offen sc1\n"
  "s_barrier\n"
  "s_waitcnt vmcnt(10)\n"
  "v_mfma_f32_32x32x16_bf16 v[0:15], v[88:91], v[80:83], v[0:15]\n"
  "s_add_u32 s41, s60, 23552\n"
  "buffer_load_dwordx4 v[140:143], v160, s[16:19], s41 offen sc1\n"
  "v_mfma_f32_32x32x16_bf16 v[16:31], v[92:95], v[80:83], v[16:31]\n"
  "s_add_u32 s41, s62, 482816\n"
  "buffer_load_dwordx4 v[148:151], v161, s[16:19], s41 offen sc1\n"
  "v_mfma_f32_32x32x16_bf16 v[32:47], v[96:99], v[80:83], v[32:47]\n"
  "s_add_u32 s41, s63, 482816\n"
  "buffer_load_dwordx4 v[152:155], v161, s[16:19], s41 offen sc1\n"
  "v_mfma_f32_32x32x16_bf16 v[48:63], v[88:91], v[84:87], v[48:63]\n"
  "s_add_u32 s41, s64, 482816\n"
  "buffer_load_dwordx4 v[156:159], v161, s[16:19], s41 offen sc1\n"
  "v_mfma_f32_32x32x16_bf16 v[64:79], v[92:95], v[84:87], v[64:79]\n"
  "s_add_u32 s41, s61, 23552\n"
  "buffer_load_dwordx4 v[144:147], v160, s[16:19], s41 offen sc1\n"
  "s_barrier\n"
  "s_waitcnt vmcnt(10)\n"
  "v_mfma_f32_32x32x16_bf16 v[0:15], v[108:111], v[100:103], v[0:15]\n"
  "v_mfma_f32_32x32x16_bf16 v[16:31], v[112:115], v[100:103], v[16:31]\n"
  "v_mfma_f32_32x32x16_bf16 v[32:47], v[116:119], v[100:103], v[32:47]\n"
  "v_mfma_f32_32x32x16_bf16 v[48:63], v[108:111], v[104:107], v[48:63]\n"
  "v_mfma_f32_32x32x16_bf16 v[64:79], v[112:115], v[104:107], v[64:79]\n"
  "s_waitcnt vmcnt(5)\n"
  "v_mfma_f32_32x32x16_bf16 v[0:15], v[128:131], v[120:123], v[0:15]\n"
  "v_mfma_f32_32x32x16_bf16 v[16:31], v[132:135], v[120:123], v[16:31]\n"
  "v_mfma_f32_32x32x16_bf16 v[32:47], v[136:139], v[120:123], v[32:47]\n"
  "v_mfma_f32_32x32x16_bf16 v[48:63], v[128:131], v[124:127], v[48:63]\n"
  "v_mfma_f32_32x32x16_bf16 v[64:79], v[132:135], v[124:127], v[64:79]\n"
  "s_waitcnt vmcnt(0)\n"
  "v_mfma_f32_32x32x16_bf16 v[0:15], v[148:151], v[140:143], v[0:15]\n"
  "v_mfma_f32_32x32x16_bf16 v[16:31], v[152:155], v[140:143], v[16:31]\n"
  "v_mfma_f32_32x32x16_bf16 v[32:47], v[156:159], v[140:143], v[32:47]\n"
  "v_mfma_f32_32x32x16_bf16 v[48:63], v[148:151], v[144:147], v[48:63]\n"
  "v_mfma_f32_32x32x16_bf16 v[64:79], v[152:155], v[144:147], v[64:79]\n"
  "s_setprio 3\n"
  "v_add_u32 v161, s78, v160\n"
  "v_add_u32 v162, s79, v160\n"
  "v_add_u32 v163, s80, v160\n"
  "s_nop 8\n"
  "ds_write_b128 v161, v[8:11] offset:0\n"
  "ds_write_b128 v161, v[12:15] offset:20480\n"
  "ds_write_b128 v161, v[24:27] offset:1024\n"
  "ds_write_b128 v161, v[28:31] offset:21504\n"
  "ds_write_b128 v161, v[40:43] offset:2048\n"
  "ds_write_b128 v161, v[44:47] offset:22528\n"
  "ds_write_b128 v161, v[56:59] offset:3072\n"
  "ds_write_b128 v161, v[60:63] offset:23552\n"
  "ds_write_b128 v161, v[72:75] offset:4096\n"
  "ds_write_b128 v161, v[76:79] offset:24576\n"
  "s_waitcnt lgkmcnt(0)\n"
  "s_barrier\n"
  "ds_read_b128 v[8:11], v162 offset:0\n"
  "ds_read_b128 v[12:15], v162 offset:20480\n"
  "ds_read_b128 v[24:27], v162 offset:1024\n"
  "ds_read_b128 v[28:31], v162 offset:21504\n"
  "ds_read_b128 v[40:43], v162 offset:2048\n"
  "ds_read_b128 v[44:47], v162 offset:22528\n"
  "ds_read_b128 v[56:59], v162 offset:3072\n"
  "ds_read_b128 v[60:63], v162 offset:23552\n"
  "ds_read_b128 v[72:75], v162 offset:4096\n"
  "ds_read_b128 v[76:79], v162 offset:24576\n"
  "s_waitcnt lgkmcnt(8)\n"
  "v_pk_add_f32 v[4:5], v[4:5], v[12:13]\n"
  "v_pk_add_f32 v[6:7], v[6:7], v[14:15]\n"
  "v_pk_add_f32 v[0:1], v[0:1], v[8:9]\n"
  "v_pk_add_f32 v[2:3], v[2:3], v[10:11]\n"
  "ds_write_b128 v162, v[4:7] offset:0\n"
  "s_waitcnt lgkmcnt(7)\n"
  "v_pk_add_f32 v[20:21], v[20:21], v[28:29]\n"
  "v_pk_add_f32 v[22:23], v[22:23], v[30:31]\n"
  "v_pk_add_f32 v[16:17], v[16:17], v[24:25]\n"
  "v_pk_add_f32 v[18:19], v[18:19], v[26:27]\n"
  "ds_write_b128 v162, v[20:23] offset:1024\n"
  "s_waitcnt lgkmcnt(6)\n"
  "v_pk_add_f32 v[36:37], v[36:37], v[44:45]\n"
  "v_pk_add_f32 v[38:39], v[38:39], v[46:47]\n"
  "v_pk_add_f32 v[32:33], v[32:33], v[40:41]\n"
  "v_pk_add_f32 v[34:35], v[34:35], v[42:43]\n"
  "ds_write_b128 v162, v[36:39] offset:2048\n"
  "s_waitcnt lgkmcnt(5)\n"
  "v_pk_add_f32 v[52:53], v[52:53], v[60:61]\n"
  "v_pk_add_f32 v[54:55], v[54:55], v[62:63]\n"
  "v_pk_add_f32 v[48:49], v[48:49], v[56:57]\n"
  "v_pk_add_f32 v[50:51], v[50:51], v[58:59]\n"
  "ds_write_b128 v162, v[52:55] offset:3072\n"
  "s_waitcnt lgkmcnt(4)\n"
  "v_pk_add_f32 v[68:69], v[68:69], v[76:77]\n"
  "v_pk_add_f32 v[70:71], v[70:71], v[78:79]\n"
  "v_pk_add_f32 v[64:65], v[64:65], v[72:73]\n"
  "v_pk_add_f32 v[66:67], v[66:67], v[74:75]\n"
  "ds_write_b128 v162, v[68:71] offset:4096\n"
  "s_waitcnt lgkmcnt(0)\n"
  "s_barrier\n"
  "ds_read_b128 v[4:7], v163 offset:0\n"
  "ds_read_b128 v[20:23], v163 offset:1024\n"
  "ds_read_b128 v[36:39], v163 offset:2048\n"
  "ds_read_b128 v[52:55], v163 offset:3072\n"
  "ds_read_b128 v[68:71], v163 offset:4096\n"
  "s_waitcnt lgkmcnt(0)\n"
  "v_add_f32 v0, v0, v4\n"
  "v_add_f32 v1, v1, v5\n"
  "v_add_f32 v2, v2, v6\n"
  "v_add_f32 v3, v3, v7\n"
  "v_mul_f32 v4, 0xbfb8aa3b, v0\n"
  "v_mul_f32 v5, 0xbfb8aa3b, v1\n"
  "v_mul_f32 v6, 0xbfb8aa3b, v2\n"
  "v_mul_f32 v7, 0xbfb8aa3b, v3\n"
  "v_exp_f32 v4, v4\n"
  "v_exp_f32 v5, v5\n"
  "v_exp_f32 v6, v6\n"
  "v_exp_f32 v7, v7\n"
  "v_add_f32 v4, 1.0, v4\n"
  "v_add_f32 v5, 1.0, v5\n"
  "v_add_f32 v6, 1.0, v6\n"
  "v_add_f32 v7, 1.0, v7\n"
  "v_rcp_f32 v4, v4\n"
  "v_rcp_f32 v5, v5\n"
  "v_rcp_f32 v6, v6\n"
  "v_rcp_f32 v7, v7\n"
  "v_mul_f32 v0, v0, v4\n"
  "v_mul_f32 v1, v1, v5\n"
  "v_mul_f32 v2, v2, v6\n"
  "v_mul_f32 v3, v3, v7\n"
  "ds_write_b128 v162, v[0:3] offset:20480\n"
  "v_add_f32 v16, v16, v20\n"
  "v_add_f32 v17, v17, v21\n"
  "v_add_f32 v18, v18, v22\n"
  "v_add_f32 v19, v19, v23\n"
  "v_mul_f32 v20, 0xbfb8aa3b, v16\n"
  "v_mul_f32 v21, 0xbfb8aa3b, v17\n"
  "v_mul_f32 v22, 0xbfb8aa3b, v18\n"
  "v_mul_f32 v23, 0xbfb8aa3b, v19\n"
  "v_exp_f32 v20, v20\n"
  "v_exp_f32 v21, v21\n"
  "v_exp_f32 v22, v22\n"
  "v_exp_f32 v23, v23\n"
  "v_add_f32 v20, 1.0, v20\n"
  "v_add_f32 v21, 1.0, v21\n"
  "v_add_f32 v22, 1.0, v22\n"
  "v_add_f32 v23, 1.0, v23\n"
  "v_rcp_f32 v20, v20\n"
  "v_rcp_f32 v21, v21\n"
  "v_rcp_f32 v22, v22\n"
  "v_rcp_f32 v23, v23\n"
  "v_mul_f32 v16, v16, v20\n"
  "v_mul_f32 v17, v17, v21\n"
  "v_mul_f32 v18, v18, v22\n"
  "v_mul_f32 v19, v19, v23\n"
  "ds_write_b128 v162, v[16:19] offset:21504\n"
  "s_barrier\n"
  "v_add_f32 v32, v32, v36\n"
  "v_add_f32 v33, v33, v37\n"
  "v_add_f32 v34, v34, v38\n"
  "v_add_f32 v35, v35, v39\n"
  "v_mul_f32 v36, 0xbfb8aa3b, v32\n"
  "v_mul_f32 v37, 0xbfb8aa3b, v33\n"
  "v_mul_f32 v38, 0xbfb8aa3b, v34\n"
  "v_mul_f32 v39, 0xbfb8aa3b, v35\n"
  "v_exp_f32 v36, v36\n"
  "v_exp_f32 v37, v37\n"
  "v_exp_f32 v38, v38\n"
  "v_exp_f32 v39, v39\n"
  "v_add_f32 v36, 1.0, v36\n"
  "v_add_f32 v37, 1.0, v37\n"
  "v_add_f32 v38, 1.0, v38\n"
  "v_add_f32 v39, 1.0, v39\n"
  "v_rcp_f32 v36, v36\n"
  "v_rcp_f32 v37, v37\n"
  "v_rcp_f32 v38, v38\n"
  "v_rcp_f32 v39, v39\n"
  "v_mul_f32 v32, v32, v36\n"
  "v_mul_f32 v33, v33, v37\n"
  "v_mul_f32 v34, v34, v38\n"
  "v_mul_f32 v35, v35, v39\n"
  "ds_write_b128 v162, v[32:35] offset:22528\n"
  "v_add_f32 v48, v48, v52\n"
  "v_add_f32 v49, v49, v53\n"
  "v_add_f32 v50, v50, v54\n"
  "v_add_f32 v51, v51, v55\n"
  "v_mul_f32 v52, 0xbfb8aa3b, v48\n"
  "v_mul_f32 v53, 0xbfb8aa3b, v49\n"
  "v_mul_f32 v54, 0xbfb8aa3b, v50\n"
  "v_mul_f32 v55, 0xbfb8aa3b, v51\n"
  "v_exp_f32 v52, v52\n"
  "v_exp_f32 v53, v53\n"
  "v_exp_f32 v54, v54\n"
  "v_exp_f32 v55, v55\n"
  "v_add_f32 v52, 1.0, v52\n"
  "v_add_f32 v53, 1.0, v53\n"
  "v_add_f32 v54, 1.0, v54\n"
  "v_add_f32 v55, 1.0, v55\n"
  "v_rcp_f32 v52, v52\n"
  "v_rcp_f32 v53, v53\n"
  "v_rcp_f32 v54, v54\n"
  "v_rcp_f32 v55, v55\n"
  "v_mul_f32 v48, v48, v52\n"
  "v_mul_f32 v49, v49, v53\n"
  "v_mul_f32 v50, v50, v54\n"
  "v_mul_f32 v51, v51, v55\n"
  "ds_write_b128 v162, v[48:51] offset:23552\n"
  "v_add_f32 v64, v64, v68\n"
  "v_add_f32 v65, v65, v69\n"
  "v_add_f32 v66, v66, v70\n"
  "v_add_f32 v67, v67, v71\n"
  "v_mul_f32 v68, 0xbfb8aa3b, v64\n"
  "v_mul_f32 v69, 0xbfb8aa3b, v65\n"
  "v_mul_f32 v70, 0xbfb8aa3b, v66\n"
  "v_mul_f32 v71, 0xbfb8aa3b, v67\n"
  "v_exp_f32 v68, v68\n"
  "v_exp_f32 v69, v69\n"
  "v_exp_f32 v70, v70\n"
  "v_exp_f32 v71, v71\n"
  "v_add_f32 v68, 1.0, v68\n"
  "v_add_f32 v69, 1.0, v69\n"
  "v_add_f32 v70, 1.0, v70\n"
  "v_add_f32 v71, 1.0, v71\n"
  "v_rcp_f32 v68, v68\n"
  "v_rcp_f32 v69, v69\n"
  "v_rcp_f32 v70, v70\n"
  "v_rcp_f32 v71, v71\n"
  "v_mul_f32 v64, v64, v68\n"
  "v_mul_f32 v65, v65, v69\n"
  "v_mul_f32 v66, v66, v70\n"
  "v_mul_f32 v67, v67, v71\n"
  "ds_write_b128 v162, v[64:67] offset:24576\n"
  "s_waitcnt lgkmcnt(0)\n"
  "s_barrier\n"
  "s_endpgm\n"
  "50:\n"
  "v_lshrrev_b32 v2, 5, v0\n"
  "v_bfe_u32 v3, v0, 2, 2\n"
  "v_lshl_or_b32 v152, v2, 3, v3\n"
  "v_bfe_u32 v4, v0, 1, 3\n"
  "v_xor_b32 v2, v2, v4\n"
  "v_and_b32 v4, 31, v0\n"
  "v_lshlrev_b32 v4, 7, v4\n"
  "v_lshl_or_b32 v6, v2, 4, v4\n"
  "v_bfe_u32 v2, v0, 4, 1\n"
  "v_and_b32 v4, 15, v0\n"
  "v_cmp_eq_u32 vcc, v2, v4\n"
  "v_and_b32 v4, 1, v0\n"
  "v_lshlrev_b32 v4, 1, v4\n"
  "v_lshl_or_b32 v2, v2, 2, v4\n"
  "v_bfe_u32 v4, v0, 1, 1\n"
  "v_or_b32 v2, v2, v4\n"
  "v_mov_b32 v4, 0x3f803f80\n"
  "v_lshlrev_b32 v153, 3, v2\n"
  "v_lshlrev_b32 v2, 2, v2\n"
  "v_cndmask_b32 v140, 0, v4, vcc\n"
  "v_cndmask_b32 v141, 0, v4, vcc\n"
  "v_cndmask_b32 v142, 0, v4, vcc\n"
  "v_cndmask_b32 v143, 0, v4, vcc\n"
  "v_lshlrev_b32 v3, 6, v3\n"
  "v_lshl_or_b32 v3, v152, 8, v3\n"
  "v_and_b32 v7, 16, v156\n"
  "v_bfe_u32 v8, v156, 8, 2\n"
  "v_lshlrev_b32 v8, 4, v8\n"
  "v_lshl_add_u32 v7, v7, 2, v8\n"
  "v_and_b32 v8, 0x1f0, v156\n"
  "s_mov_b32 s36, 0\n"
  "s_mov_b32 s37, -1\n"
  "s_mov_b32 s38, 0xffff0000\n"
  "s_mov_b32 s39, 0xffff0000\n"
  "s_mov_b32 s48, 0xeeee0000\n"
  "s_mov_b32 s49, 0xeeee0000\n"
  "s_waitcnt vmcnt(0)\n"
  "v_lshlrev_b32 v38, 16, v84\n"
  "v_and_b32 v39, 0xffff0000, v84\n"
  "v_lshlrev_b32 v40, 16, v85\n"
  "v_and_b32 v41, 0xffff0000, v85\n"
  "v_lshlrev_b32 v42, 16, v86\n"
  "v_and_b32 v43, 0xffff0000, v86\n"
  "v_lshlrev_b32 v44, 16, v87\n"
  "v_and_b32 v45, 0xffff0000, v87\n"
  "v_lshlrev_b32 v46, 16, v88\n"
  "v_and_b32 v47, 0xffff0000, v88\n"
  "v_pk_add_f32 v[32:33], v[38:39], v[40:41]\n"
  "v_pk_add_f32 v[34:35], v[42:43], v[44:45]\n"
  "v_pk_add_f32 v[32:33], v[32:33], v[46:47]\n"
  "v_pk_add_f32 v[32:33], v[32:33], v[34:35]\n"
  "v_add_f32 v32, v32, v33\n"
  "s_nop 1\n"
  "v_add_f32_dpp v32, v32, v32 row_ror:1 row_mask:0xf bank_mask:0xf\n"
  "s_nop 1\n"
  "v_add_f32_dpp v32, v32, v32 row_ror:2 row_mask:0xf bank_mask:0xf\n"
  "s_nop 1\n"
  "v_add_f32_dpp v32, v32, v32 row_ror:4 row_mask:0xf bank_mask:0xf\n"
  "s_nop 1\n"
  "v_add_f32_dpp v32, v32, v32 row_ror:8 row_mask:0xf bank_mask:0xf\n"
  "v_mov_b32 v33, v32\n"
  "s_nop 1\n"
  "v_permlane16_swap_b32 v32, v33\n"
  "v_add_f32 v32, v32, v33\n"
  "v_mov_b32 v33, v32\n"
  "s_nop 1\n"
  "v_permlane32_swap_b32 v32, v33\n"
  "v_add_f32 v32, v32, v33\n"
  "v_mul_f32 v48, 0x3acccccd, v32\n"
  "v_add_u32 v49, 1, v90\n"
  "v_add_u32 v51, 1, v91\n"
  "v_pk_add_f32 v[38:39], v[38:39], v[48:49] op_sel_hi:[1,0] neg_lo:[0,1] neg_hi:[0,1]\n"
  "v_pk_add_f32 v[40:41], v[40:41], v[48:49] op_sel_hi:[1,0] neg_lo:[0,1] neg_hi:[0,1]\n"
  "v_pk_add_f32 v[42:43], v[42:43], v[48:49] op_sel_hi:[1,0] neg_lo:[0,1] neg_hi:[0,1]\n"
  "v_pk_add_f32 v[44:45], v[44:45], v[48:49] op_sel_hi:[1,0] neg_lo:[0,1] neg_hi:[0,1]\n"
  "v_pk_add_f32 v[46:47], v[46:47], v[48:49] op_sel_hi:[1,0] neg_lo:[0,1] neg_hi:[0,1]\n"
  "v_pk_mul_f32 v[32:33], v[38:39], v[38:39]\n"
  "v_pk_fma_f32 v[32:33], v[40:41], v[40:41], v[32:33]\n"
  "v_pk_fma_f32 v[32:33], v[42:43], v[42:43], v[32:33]\n"
  "v_pk_fma_f32 v[32:33], v[44:45], v[44:45], v[32:33]\n"
  "v_pk_fma_f32 v[32:33], v[46:47], v[46:47], v[32:33]\n"
  "v_add_f32 v32, v32, v33\n"
  "s_nop 1\n"
  "v_add_f32_dpp v32, v32, v32 row_ror:1 row_mask:0xf bank_mask:0xf\n"
  "s_nop 1\n"
  "v_add_f32_dpp v32, v32, v32 row_ror:2 row_mask:0xf bank_mask:0xf\n"
  "s_nop 1\n"
  "v_add_f32_dpp v32, v32, v32 row_ror:4 row_mask:0xf bank_mask:0xf\n"
  "s_nop 1\n"
  "v_add_f32_dpp v32, v32, v32 row_ror:8 row_mask:0xf bank_mask:0xf\n"
  "v_mov_b32 v33, v32\n"
  "s_nop 1\n"
  "v_permlane16_swap_b32 v32, v33\n"
  "v_add_f32 v32, v32, v33\n"
  "v_mov_b32 v33, v32\n"
  "s_nop 1\n"
  "v_permlane32_swap_b32 v32, v33\n"
  "v_add_f32 v32, v32, v33\n"
  "v_mov_b32 v50, 0x3727c5ac\n"
  "v_fmac_f32 v50, 0x3acccccd, v32\n"
  "v_rsq_f32 v50, v50\n"
  "v_readfirstlane_b32 s46, v49\n"
  "v_readfirstlane_b32 s47, v51\n"
  "s_waitcnt lgkmcnt(0)\n"
  "s_mov_b64 exec, 1\n"
  "buffer_store_dwordx4 v[48:51], off, s[16:19], s63 sc1\n"
  "s_mov_b64 exec, -1\n"
  "v_xor_b32 v144, s70, v6\n"
  "v_xor_b32 v145, 32, v144\n"
  "v_xor_b32 v146, 64, v144\n"
  "v_xor_b32 v147, 96, v144\n"
  "v_add_u32 v148, s73, v7\n"
  "v_add_u32 v150, s75, v8\n"
  "s_mov_b64 s[44:45], 0\n"
  "s_cmp_lg_u32 s5, 0\n"
  "s_cbranch_scc1 1f\n"
  "v_lshrrev_b32 v4, v2, s52\n"
  "v_lshrrev_b32 v2, v2, s53\n"
  "v_xor_b32 v3, s71, v3\n"
  "v_and_b32 v4, 7, v4\n"
  "v_and_b32 v2, 7, v2\n"
  "v_xor_b32 v5, 64, v3\n"
  "v_lshl_add_u32 v64, v4, 3, v3\n"
  "v_lshl_add_u32 v68, v2, 3, v5\n"
  "v_xor_b32 v65, 4096, v64\n"
  "v_xor_b32 v69, 4096, v68\n"
  "v_xor_b32 v66, 8192, v64\n"
  "v_xor_b32 v70, 8192, v68\n"
  "v_xor_b32 v67, 12288, v64\n"
  "v_xor_b32 v71, 12288, v68\n"
  "s_barrier\n"
  "ds_read_b128 v[84:87], v144 offset:0\n"
  "ds_read_b64_tr_b16 v[92:93], v64 offset:32768\n"
  "ds_read_b64_tr_b16 v[94:95], v64 offset:33792\n"
  "ds_read_b64_tr_b16 v[96:97], v68 offset:32768\n"
  "ds_read_b64_tr_b16 v[98:99], v68 offset:33792\n"
  "ds_read_b128 v[88:91], v144 offset:20480\n"
  "ds_read_b128 v[100:103], v145 offset:0\n"
  "ds_read_b64_tr_b16 v[108:109], v65 offset:32768\n"
  "ds_read_b64_tr_b16 v[110:111], v65 offset:33792\n"
  "ds_read_b64_tr_b16 v[112:113], v69 offset:32768\n"
  "ds_read_b64_tr_b16 v[114:115], v69 offset:33792\n"
  "ds_read_b128 v[104:107], v145 offset:20480\n"
  "s_waitcnt lgkmcnt(6)\n"
  "v_mfma_f32_32x32x16_bf16 v[0:15], v[92:95], v[84:87], 0\n"
  "ds_read_b128 v[116:119], v146 offset:0\n"
  "ds_read_b64_tr_b16 v[124:125], v66 offset:32768\n"
  "v_lshl_add_u32 v151, v152, 7, v153\n"
  "v_add_u32 v151, s74, v151\n"
  "v_mfma_f32_32x32x16_bf16 v[16:31], v[96:99], v[84:87], 0\n"
  "ds_read_b64_tr_b16 v[126:127], v66 offset:33792\n"
  "ds_read_b64_tr_b16 v[128:129], v70 offset:32768\n"
  "v_lshrrev_b32 v149, 2, v156\n"
  "v_add_u32 v149, s72, v149\n"
  "v_mfma_f32_32x32x16_bf16 v[32:47], v[92:95], v[88:91], 0\n"
  "ds_read_b64_tr_b16 v[130:131], v70 offset:33792\n"
  "ds_read_b128 v[120:123], v146 offset:20480\n"
  "v_mfma_f32_32x32x16_bf16 v[48:63], v[96:99], v[88:91], 0\n"
  "v_mfma_f32_16x16x32_bf16 v[132:135], v[92:95], v[140:143], 0\n"
  "v_mfma_f32_16x16x32_bf16 v[136:139], v[96:99], v[140:143], 0\n"
  "s_waitcnt lgkmcnt(6)\n"
  "v_mfma_f32_32x32x16_bf16 v[0:15], v[108:111], v[100:103], v[0:15]\n"
  "ds_read_b128 v[84:87], v147 offset:0\n"
  "ds_read_b64_tr_b16 v[92:93], v67 offset:32768\n"
  "v_mfma_f32_32x32x16_bf16 v[16:31], v[112:115], v[100:103], v[16:31]\n"
  "ds_read_b64_tr_b16 v[94:95], v67 offset:33792\n"
  "ds_read_b64_tr_b16 v[96:97], v71 offset:32768\n"
  "v_mfma_f32_32x32x16_bf16 v[32:47], v[108:111], v[104:107], v[32:47]\n"
  "ds_read_b64_tr_b16 v[98:99], v71 offset:33792\n"
  "ds_read_b128 v[88:91], v147 offset:20480\n"
  "v_mfma_f32_32x32x16_bf16 v[48:63], v[112:115], v[104:107], v[48:63]\n"
  "v_mfma_f32_16x16x32_bf16 v[132:135], v[108:111], v[140:143], v[132:135]\n"
  "v_mfma_f32_16x16x32_bf16 v[136:139], v[112:115], v[140:143], v[136:139]\n"
  "s_waitcnt lgkmcnt(6)\n"
  "v_mfma_f32_32x32x16_bf16 v[0:15], v[124:127], v[116:119], v[0:15]\n"
  "s_barrier\n"
  "v_mfma_f32_32x32x16_bf16 v[16:31], v[128:131], v[116:119], v[16:31]\n"
  "ds_read_b128 v[100:103], v144 offset:4096\n"
  "ds_read_b64_tr_b16 v[108:109], v64 offset:49152\n"
  "v_mfma_f32_32x32x16_bf16 v[32:47], v[124:127], v[120:123], v[32:47]\n"
  "ds_read_b64_tr_b16 v[110:111], v64 offset:50176\n"
  "ds_read_b64_tr_b16 v[112:113], v68 offset:49152\n"
  "v_mfma_f32_32x32x16_bf16 v[48:63], v[128:131], v[120:123], v[48:63]\n"
  "ds_read_b64_tr_b16 v[114:115], v68 offset:50176\n"
  "ds_read_b128 v[104:107], v144 offset:24576\n"
  "s_waitcnt lgkmcnt(6)\n"
  "v_mfma_f32_32x32x16_bf16 v[0:15], v[92:95], v[84:87], v[0:15]\n"
  "ds_read_b128 v[116:119], v145 offset:4096\n"
  "ds_read_b64_tr_b16 v[124:125], v65 offset:49152\n"
  "v_mfma_f32_32x32x16_bf16 v[16:31], v[96:99], v[84:87], v[16:31]\n"
  "ds_read_b64_tr_b16 v[126:127], v65 offset:50176\n"
  "ds_read_b64_tr_b16 v[128:129], v69 offset:49152\n"
  "v_mfma_f32_32x32x16_bf16 v[32:47], v[92:95], v[88:91], v[32:47]\n"
  "ds_read_b64_tr_b16 v[130:131], v69 offset:50176\n"
  "ds_read_b128 v[120:123], v145 offset:24576\n"
  "v_mfma_f32_32x32x16_bf16 v[48:63], v[96:99], v[88:91], v[48:63]\n"
  "s_waitcnt lgkmcnt(6)\n"
  "v_mfma_f32_32x32x16_bf16 v[0:15], v[108:111], v[100:103], v[0:15]\n"
  "ds_read_b128 v[84:87], v146 offset:4096\n"
  "ds_read_b64_tr_b16 v[92:93], v66 offset:49152\n"
  "v_mfma_f32_32x32x16_bf16 v[16:31], v[112:115], v[100:103], v[16:31]\n"
  "ds_read_b64_tr_b16 v[94:95], v66 offset:50176\n"
  "ds_read_b64_tr_b16 v[96:97], v70 offset:49152\n"
  "v_mfma_f32_32x32x16_bf16 v[32:47], v[108:111], v[104:107], v[32:47]\n"
  "ds_read_b64_tr_b16 v[98:99], v70 offset:50176\n"
  "ds_read_b128 v[88:91], v146 offset:24576\n"
  "v_mfma_f32_32x32x16_bf16 v[48:63], v[112:115], v[104:107], v[48:63]\n"
  "v_mfma_f32_16x16x32_bf16 v[132:135], v[108:111], v[140:143], v[132:135]\n"
  "v_mfma_f32_16x16x32_bf16 v[136:139], v[112:115], v[140:143], v[136:139]\n"
  "s_waitcnt lgkmcnt(6)\n"
  "v_mfma_f32_32x32x16_bf16 v[0:15], v[124:127], v[116:119], v[0:15]\n"
  "ds_read_b128 v[100:103], v147 offset:4096\n"
  "ds_read_b64_tr_b16 v[108:109], v67 offset:49152\n"
  "v_mfma_f32_32x32x16_bf16 v[16:31], v[128:131], v[116:119], v[16:31]\n"
  "ds_read_b64_tr_b16 v[110:111], v67 offset:50176\n"
  "ds_read_b64_tr_b16 v[112:113], v71 offset:49152\n"
  "v_mfma_f32_32x32x16_bf16 v[32:47], v[124:127], v[120:123], v[32:47]\n"
  "ds_read_b64_tr_b16 v[114:115], v71 offset:50176\n"
  "ds_read_b128 v[104:107], v147 offset:24576\n"
  "v_mfma_f32_32x32x16_bf16 v[48:63], v[128:131], v[120:123], v[48:63]\n"
  "v_mfma_f32_16x16x32_bf16 v[132:135], v[124:127], v[140:143], v[132:135]\n"
  "v_mfma_f32_16x16x32_bf16 v[136:139], v[128:131], v[140:143], v[136:139]\n"
  "s_waitcnt lgkmcnt(6)\n"
  "v_mfma_f32_32x32x16_bf16 v[0:15], v[92:95], v[84:87], v[0:15]\n"
  "s_barrier\n"
  "v_mfma_f32_32x32x16_bf16 v[16:31], v[96:99], v[84:87], v[16:31]\n"
  "ds_read_b128 v[116:119], v144 offset:8192\n"
  "ds_read_b64_tr_b16 v[124:125], v64 offset:0\n"
  "v_mfma_f32_32x32x16_bf16 v[32:47], v[92:95], v[88:91], v[32:47]\n"
  "ds_read_b64_tr_b16 v[126:127], v64 offset:1024\n"
  "ds_read_b64_tr_b16 v[128:129], v68 offset:0\n"
  "v_mfma_f32_32x32x16_bf16 v[48:63], v[96:99], v[88:91], v[48:63]\n"
  "ds_read_b64_tr_b16 v[130:131], v68 offset:1024\n"
  "ds_read_b128 v[120:123], v144 offset:28672\n"
  "s_waitcnt lgkmcnt(6)\n"
  "v_mfma_f32_32x32x16_bf16 v[0:15], v[108:111], v[100:103], v[0:15]\n"
  "ds_read_b128 v[84:87], v145 offset:8192\n"
  "ds_read_b64_tr_b16 v[92:93], v65 offset:0\n"
  "v_mfma_f32_32x32x16_bf16 v[16:31], v[112:115], v[100:103], v[16:31]\n"
  "ds_read_b64_tr_b16 v[94:95], v65 offset:1024\n"
  "ds_read_b64_tr_b16 v[96:97], v69 offset:0\n"
  "v_mfma_f32_32x32x16_bf16 v[32:47], v[108:111], v[104:107], v[32:47]\n"
  "ds_read_b64_tr_b16 v[98:99], v69 offset:1024\n"
  "ds_read_b128 v[88:91], v145 offset:28672\n"
  "v_mfma_f32_32x32x16_bf16 v[48:63], v[112:115], v[104:107], v[48:63]\n"
  "s_waitcnt lgkmcnt(6)\n"
  "v_mfma_f32_32x32x16_bf16 v[0:15], v[124:127], v[116:119], v[0:15]\n"
  "ds_read_b128 v[100:103], v146 offset:8192\n"
  "ds_read_b64_tr_b16 v[108:109], v66 offset:0\n"
  "v_mfma_f32_32x32x16_bf16 v[16:31], v[128:131], v[116:119], v[16:31]\n"
  "ds_read_b64_tr_b16 v[110:111], v66 offset:1024\n"
  "ds_read_b64_tr_b16 v[112:113], v70 offset:0\n"
  "v_mfma_f32_32x32x16_bf16 v[32:47], v[124:127], v[120:123], v[32:47]\n"
  "ds_read_b64_tr_b16 v[114:115], v70 offset:1024\n"
  "ds_read_b128 v[104:107], v146 offset:28672\n"
  "v_mfma_f32_32x32x16_bf16 v[48:63], v[128:131], v[120:123], v[48:63]\n"
  "v_mfma_f32_16x16x32_bf16 v[132:135], v[124:127], v[140:143], v[132:135]\n"
  "v_mfma_f32_16x16x32_bf16 v[136:139], v[128:131], v[140:143], v[136:139]\n"
  "s_waitcnt lgkmcnt(6)\n"
  "v_mfma_f32_32x32x16_bf16 v[0:15], v[92:95], v[84:87], v[0:15]\n"
  "ds_read_b128 v[116:119], v147 offset:8192\n"
  "ds_read_b64_tr_b16 v[124:125], v67 offset:0\n"
  "v_mfma_f32_32x32x16_bf16 v[16:31], v[96:99], v[84:87], v[16:31]\n"
  "ds_read_b64_tr_b16 v[126:127], v67 offset:1024\n"
  "ds_read_b64_tr_b16 v[128:129], v71 offset:0\n"
  "v_mfma_f32_32x32x16_bf16 v[32:47], v[92:95], v[88:91], v[32:47]\n"
  "ds_read_b64_tr_b16 v[130:131], v71 offset:1024\n"
  "ds_read_b128 v[120:123], v147 offset:28672\n"
  "v_mfma_f32_32x32x16_bf16 v[48:63], v[96:99], v[88:91], v[48:63]\n"
  "v_mfma_f32_16x16x32_bf16 v[132:135], v[92:95], v[140:143], v[132:135]\n"
  "v_mfma_f32_16x16x32_bf16 v[136:139], v[96:99], v[140:143], v[136:139]\n"
  "s_waitcnt lgkmcnt(6)\n"
  "v_mfma_f32_32x32x16_bf16 v[0:15], v[108:111], v[100:103], v[0:15]\n"
  "s_barrier\n"
  "v_mfma_f32_32x32x16_bf16 v[16:31], v[112:115], v[100:103], v[16:31]\n"
  "ds_read_b128 v[84:87], v144 offset:12288\n"
  "ds_read_b64_tr_b16 v[92:93], v64 offset:16384\n"
  "v_mfma_f32_32x32x16_bf16 v[32:47], v[108:111], v[104:107], v[32:47]\n"
  "ds_read_b64_tr_b16 v[94:95], v64 offset:17408\n"
  "ds_read_b64_tr_b16 v[96:97], v68 offset:16384\n"
  "v_mfma_f32_32x32x16_bf16 v[48:63], v[112:115], v[104:107], v[48:63]\n"
  "ds_read_b64_tr_b16 v[98:99], v68 offset:17408\n"
  "ds_read_b128 v[88:91], v144 offset:32768\n"
  "s_waitcnt lgkmcnt(6)\n"
  "v_mfma_f32_32x32x16_bf16 v[0:15], v[124:127], v[116:119], v[0:15]\n"
  "ds_read_b128 v[100:103], v145 offset:12288\n"
  "ds_read_b64_tr_b16 v[108:109], v65 offset:16384\n"
  "v_mfma_f32_32x32x16_bf16 v[16:31], v[128:131], v[116:119], v[16:31]\n"
  "ds_read_b64_tr_b16 v[110:111], v65 offset:17408\n"
  "ds_read_b64_tr_b16 v[112:113], v69 offset:16384\n"
  "v_mfma_f32_32x32x16_bf16 v[32:47], v[124:127], v[120:123], v[32:47]\n"
  "ds_read_b64_tr_b16 v[114:115], v69 offset:17408\n"
  "ds_read_b128 v[104:107], v145 offset:32768\n"
  "v_mfma_f32_32x32x16_bf16 v[48:63], v[128:131], v[120:123], v[48:63]\n"
  "s_waitcnt lgkmcnt(6)\n"
  "ds_read_b64_tr_b16 v[74:75], v151 offset:0\n"
  "ds_read_b64_tr_b16 v[76:77], v151 offset:512\n"
  "ds_read_b64_tr_b16 v[78:79], v151 offset:64\n"
  "ds_read_b64_tr_b16 v[80:81], v151 offset:576\n"
  "v_mfma_f32_32x32x16_bf16 v[0:15], v[92:95], v[84:87], v[0:15]\n"
  "ds_read_b128 v[116:119], v146 offset:12288\n"
  "ds_read_b64_tr_b16 v[124:125], v66 offset:16384\n"
  "v_mfma_f32_32x32x16_bf16 v[16:31], v[96:99], v[84:87], v[16:31]\n"
  "ds_read_b64_tr_b16 v[126:127], v66 offset:17408\n"
  "ds_read_b64_tr_b16 v[128:129], v70 offset:16384\n"
  "v_mfma_f32_32x32x16_bf16 v[32:47], v[92:95], v[88:91], v[32:47]\n"
  "ds_read_b64_tr_b16 v[130:131], v70 offset:17408\n"
  "s_waitcnt lgkmcnt(12)\n"
  "ds_read_b128 v[120:123], v146 offset:32768\n"
  "v_mfma_f32_32x32x16_bf16 v[48:63], v[96:99], v[88:91], v[48:63]\n"
  "v_mfma_f32_16x16x32_bf16 v[132:135], v[92:95], v[140:143], v[132:135]\n"
  "v_mfma_f32_16x16x32_bf16 v[136:139], v[96:99], v[140:143], v[136:139]\n"
  "s_waitcnt lgkmcnt(6)\n"
  "v_mfma_f32_32x32x16_bf16 v[0:15], v[108:111], v[100:103], v[0:15]\n"
  "ds_read_b128 v[84:87], v147 offset:12288\n"
  "ds_read_b64_tr_b16 v[92:93], v67 offset:16384\n"
  "v_mfma_f32_32x32x16_bf16 v[16:31], v[112:115], v[100:103], v[16:31]\n"
  "ds_read_b64_tr_b16 v[94:95], v67 offset:17408\n"
  "ds_read_b64_tr_b16 v[96:97], v71 offset:16384\n"
  "v_mfma_f32_32x32x16_bf16 v[32:47], v[108:111], v[104:107], v[32:47]\n"
  "ds_read_b64_tr_b16 v[98:99], v71 offset:17408\n"
  "ds_read_b128 v[88:91], v147 offset:32768\n"
  "v_mfma_f32_32x32x16_bf16 v[48:63], v[112:115], v[104:107], v[48:63]\n"
  "v_mfma_f32_16x16x32_bf16 v[132:135], v[108:111], v[140:143], v[132:135]\n"
  "v_mfma_f32_16x16x32_bf16 v[136:139], v[112:115], v[140:143], v[136:139]\n"
  "s_waitcnt lgkmcnt(6)\n"
  "v_mfma_f32_32x32x16_bf16 v[0:15], v[124:127], v[116:119], v[0:15]\n"
  "s_barrier\n"
  "v_mfma_f32_32x32x16_bf16 v[16:31], v[128:131], v[116:119], v[16:31]\n"
  "ds_read_b128 v[100:103], v144 offset:16384\n"
  "ds_read_b64_tr_b16 v[108:109], v64 offset:32768\n"
  "v_mfma_f32_32x32x16_bf16 v[32:47], v[124:127], v[120:123], v[32:47]\n"
  "ds_read_b64_tr_b16 v[110:111], v64 offset:33792\n"
  "ds_read_b64_tr_b16 v[112:113], v68 offset:32768\n"
  "v_mfma_f32_32x32x16_bf16 v[48:63], v[128:131], v[120:123], v[48:63]\n"
  "ds_read_b64_tr_b16 v[114:115], v68 offset:33792\n"
  "ds_read_b128 v[104:107], v144 offset:36864\n"
  "s_waitcnt lgkmcnt(6)\n"
  "v_mfma_f32_32x32x16_bf16 v[0:15], v[92:95], v[84:87], v[0:15]\n"
  "ds_read_b128 v[116:119], v145 offset:16384\n"
  "ds_read_b64_tr_b16 v[124:125], v65 offset:32768\n"
  "v_mfma_f32_32x32x16_bf16 v[16:31], v[96:99], v[84:87], v[16:31]\n"
  "ds_read_b64_tr_b16 v[126:127], v65 offset:33792\n"
  "ds_read_b64_tr_b16 v[128:129], v69 offset:32768\n"
  "v_mfma_f32_32x32x16_bf16 v[32:47], v[92:95], v[88:91], v[32:47]\n"
  "ds_read_b64_tr_b16 v[130:131], v69 offset:33792\n"
  "ds_read_b128 v[120:123], v145 offset:36864\n"
  "v_mfma_f32_32x32x16_bf16 v[48:63], v[96:99], v[88:91], v[48:63]\n"
  "s_waitcnt lgkmcnt(6)\n"
  "v_mfma_f32_32x32x16_bf16 v[0:15], v[108:111], v[100:103], v[0:15]\n"
  "ds_read_b128 v[84:87], v146 offset:16384\n"
  "ds_read_b64_tr_b16 v[92:93], v66 offset:32768\n"
  "v_mfma_f32_32x32x16_bf16 v[16:31], v[112:115], v[100:103], v[16:31]\n"
  "ds_read_b64_tr_b16 v[94:95], v66 offset:33792\n"
  "ds_read_b64_tr_b16 v[96:97], v70 offset:32768\n"
  "v_mfma_f32_32x32x16_bf16 v[32:47], v[108:111], v[104:107], v[32:47]\n"
  "ds_read_b64_tr_b16 v[98:99], v70 offset:33792\n"
  "ds_read_b128 v[88:91], v146 offset:36864\n"
  "v_mfma_f32_32x32x16_bf16 v[48:63], v[112:115], v[104:107], v[48:63]\n"
  "v_mfma_f32_16x16x32_bf16 v[132:135], v[108:111], v[140:143], v[132:135]\n"
  "v_mfma_f32_16x16x32_bf16 v[136:139], v[112:115], v[140:143], v[136:139]\n"
  "s_waitcnt lgkmcnt(6)\n"
  "v_mfma_f32_32x32x16_bf16 v[0:15], v[124:127], v[116:119], v[0:15]\n"
  "ds_read_b128 v[100:103], v147 offset:16384\n"
  "ds_read_b64_tr_b16 v[108:109], v67 offset:32768\n"
  "v_mfma_f32_32x32x16_bf16 v[16:31], v[128:131], v[116:119], v[16:31]\n"
  "ds_read_b64_tr_b16 v[110:111], v67 offset:33792\n"
  "ds_read_b64_tr_b16 v[112:113], v71 offset:32768\n"
  "v_mfma_f32_32x32x16_bf16 v[32:47], v[124:127], v[120:123], v[32:47]\n"
  "ds_read_b64_tr_b16 v[114:115], v71 offset:33792\n"
  "ds_read_b128 v[104:107], v147 offset:36864\n"
  "v_mfma_f32_32x32x16_bf16 v[48:63], v[128:131], v[120:123], v[48:63]\n"
  "v_mfma_f32_16x16x32_bf16 v[132:135], v[124:127], v[140:143], v[132:135]\n"
  "v_mfma_f32_16x16x32_bf16 v[136:139], v[128:131], v[140:143], v[136:139]\n"
  "s_waitcnt lgkmcnt(6)\n"
  "v_mfma_f32_32x32x16_bf16 v[0:15], v[92:95], v[84:87], v[0:15]\n"
  "s_barrier\n"
  "v_mfma_f32_32x32x16_bf16 v[16:31], v[96:99], v[84:87], v[16:31]\n"
  "ds_read_b128 v[116:119], v144 offset:0\n"
  "ds_read_b64_tr_b16 v[124:125], v64 offset:49152\n"
  "v_mfma_f32_32x32x16_bf16 v[32:47], v[92:95], v[88:91], v[32:47]\n"
  "ds_read_b64_tr_b16 v[126:127], v64 offset:50176\n"
  "ds_read_b64_tr_b16 v[128:129], v68 offset:49152\n"
  "v_mfma_f32_32x32x16_bf16 v[48:63], v[96:99], v[88:91], v[48:63]\n"
  "ds_read_b64_tr_b16 v[130:131], v68 offset:50176\n"
  "ds_read_b128 v[120:123], v144 offset:20480\n"
  "s_waitcnt lgkmcnt(6)\n"
  "v_mfma_f32_32x32x16_bf16 v[0:15], v[108:111], v[100:103], v[0:15]\n"
  "ds_read_b128 v[84:87], v145 offset:0\n"
  "ds_read_b64_tr_b16 v[92:93], v65 offset:49152\n"
  "v_mfma_f32_32x32x16_bf16 v[16:31], v[112:115], v[100:103], v[16:31]\n"
  "ds_read_b64_tr_b16 v[94:95], v65 offset:50176\n"
  "ds_read_b64_tr_b16 v[96:97], v69 offset:49152\n"
  "v_mfma_f32_32x32x16_bf16 v[32:47], v[108:111], v[104:107], v[32:47]\n"
  "ds_read_b64_tr_b16 v[98:99], v69 offset:50176\n"
  "ds_read_b128 v[88:91], v145 offset:20480\n"
  "v_mfma_f32_32x32x16_bf16 v[48:63], v[112:115], v[104:107], v[48:63]\n"
  "s_waitcnt lgkmcnt(6)\n"
  "v_mfma_f32_32x32x16_bf16 v[0:15], v[124:127], v[116:119], v[0:15]\n"
  "ds_read_b128 v[100:103], v146 offset:0\n"
  "ds_read_b64_tr_b16 v[108:109], v66 offset:49152\n"
  "v_mfma_f32_32x32x16_bf16 v[16:31], v[128:131], v[116:119], v[16:31]\n"
  "ds_read_b64_tr_b16 v[110:111], v66 offset:50176\n"
  "ds_read_b64_tr_b16 v[112:113], v70 offset:49152\n"
  "v_mfma_f32_32x32x16_bf16 v[32:47], v[124:127], v[120:123], v[32:47]\n"
  "ds_read_b64_tr_b16 v[114:115], v70 offset:50176\n"
  "ds_read_b128 v[104:107], v146 offset:20480\n"
  "v_mfma_f32_32x32x16_bf16 v[48:63], v[128:131], v[120:123], v[48:63]\n"
  "v_mfma_f32_16x16x32_bf16 v[132:135], v[124:127], v[140:143], v[132:135]\n"
  "v_mfma_f32_16x16x32_bf16 v[136:139], v[128:131], v[140:143], v[136:139]\n"
  "s_waitcnt lgkmcnt(6)\n"
  "v_mfma_f32_32x32x16_bf16 v[0:15], v[92:95], v[84:87], v[0:15]\n"
  "ds_read_b128 v[116:119], v147 offset:0\n"
  "ds_read_b64_tr_b16 v[124:125], v67 offset:49152\n"
  "v_mfma_f32_32x32x16_bf16 v[16:31], v[96:99], v[84:87], v[16:31]\n"
  "ds_read_b64_tr_b16 v[126:127], v67 offset:50176\n"
  "ds_read_b64_tr_b16 v[128:129], v71 offset:49152\n"
  "v_mfma_f32_32x32x16_bf16 v[32:47], v[92:95], v[88:91], v[32:47]\n"
  "ds_read_b64_tr_b16 v[130:131], v71 offset:50176\n"
  "ds_read_b128 v[120:123], v147 offset:20480\n"
  "v_mfma_f32_32x32x16_bf16 v[48:63], v[96:99], v[88:91], v[48:63]\n"
  "v_mfma_f32_16x16x32_bf16 v[132:135], v[92:95], v[140:143], v[132:135]\n"
  "v_mfma_f32_16x16x32_bf16 v[136:139], v[96:99], v[140:143], v[136:139]\n"
  "s_waitcnt lgkmcnt(6)\n"
  "v_mfma_f32_32x32x16_bf16 v[0:15], v[108:111], v[100:103], v[0:15]\n"
  "s_barrier\n"
  "v_mfma_f32_32x32x16_bf16 v[16:31], v[112:115], v[100:103], v[16:31]\n"
  "ds_read_b128 v[84:87], v144 offset:4096\n"
  "ds_read_b64_tr_b16 v[92:93], v64 offset:0\n"
  "v_mfma_f32_32x32x16_bf16 v[32:47], v[108:111], v[104:107], v[32:47]\n"
  "ds_read_b64_tr_b16 v[94:95], v64 offset:1024\n"
  "ds_read_b64_tr_b16 v[96:97], v68 offset:0\n"
  "v_mfma_f32_32x32x16_bf16 v[48:63], v[112:115], v[104:107], v[48:63]\n"
  "ds_read_b64_tr_b16 v[98:99], v68 offset:1024\n"
  "ds_read_b128 v[88:91], v144 offset:24576\n"
  "s_waitcnt lgkmcnt(6)\n"
  "v_mfma_f32_32x32x16_bf16 v[0:15], v[124:127], v[116:119], v[0:15]\n"
  "ds_read_b128 v[100:103], v145 offset:4096\n"
  "ds_read_b64_tr_b16 v[108:109], v65 offset:0\n"
  "v_mfma_f32_32x32x16_bf16 v[16:31], v[128:131], v[116:119], v[16:31]\n"
  "ds_read_b64_tr_b16 v[110:111], v65 offset:1024\n"
  "ds_read_b64_tr_b16 v[112:113], v69 offset:0\n"
  "v_mfma_f32_32x32x16_bf16 v[32:47], v[124:127], v[120:123], v[32:47]\n"
  "ds_read_b64_tr_b16 v[114:115], v69 offset:1024\n"
  "ds_read_b128 v[104:107], v145 offset:24576\n"
  "v_mfma_f32_32x32x16_bf16 v[48:63], v[128:131], v[120:123], v[48:63]\n"
  "s_waitcnt lgkmcnt(6)\n"
  "v_mfma_f32_32x32x16_bf16 v[0:15], v[92:95], v[84:87], v[0:15]\n"
  "ds_read_b128 v[116:119], v146 offset:4096\n"
  "ds_read_b64_tr_b16 v[124:125], v66 offset:0\n"
  "v_mfma_f32_32x32x16_bf16 v[16:31], v[96:99], v[84:87], v[16:31]\n"
  "ds_read_b64_tr_b16 v[126:127], v66 offset:1024\n"
  "ds_read_b64_tr_b16 v[128:129], v70 offset:0\n"
  "v_mfma_f32_32x32x16_bf16 v[32:47], v[92:95], v[88:91], v[32:47]\n"
  "ds_read_b64_tr_b16 v[130:131], v70 offset:1024\n"
  "ds_read_b128 v[120:123], v146 offset:24576\n"
  "v_mfma_f32_32x32x16_bf16 v[48:63], v[96:99], v[88:91], v[48:63]\n"
  "v_mfma_f32_16x16x32_bf16 v[132:135], v[92:95], v[140:143], v[132:135]\n"
  "v_mfma_f32_16x16x32_bf16 v[136:139], v[96:99], v[140:143], v[136:139]\n"
  "s_waitcnt lgkmcnt(6)\n"
  "v_mfma_f32_32x32x16_bf16 v[0:15], v[108:111], v[100:103], v[0:15]\n"
  "ds_read_b128 v[84:87], v147 offset:4096\n"
  "ds_read_b64_tr_b16 v[92:93], v67 offset:0\n"
  "v_mfma_f32_32x32x16_bf16 v[16:31], v[112:115], v[100:103], v[16:31]\n"
  "ds_read_b64_tr_b16 v[94:95], v67 offset:1024\n"
  "ds_read_b64_tr_b16 v[96:97], v71 offset:0\n"
  "v_mfma_f32_32x32x16_bf16 v[32:47], v[108:111], v[104:107], v[32:47]\n"
  "ds_read_b64_tr_b16 v[98:99], v71 offset:1024\n"
  "ds_read_b128 v[88:91], v147 offset:24576\n"
  "v_mfma_f32_32x32x16_bf16 v[48:63], v[112:115], v[104:107], v[48:63]\n"
  "v_mfma_f32_16x16x32_bf16 v[132:135], v[108:111], v[140:143], v[132:135]\n"
  "v_mfma_f32_16x16x32_bf16 v[136:139], v[112:115], v[140:143], v[136:139]\n"
  "s_waitcnt lgkmcnt(6)\n"
  "v_mfma_f32_32x32x16_bf16 v[0:15], v[124:127], v[116:119], v[0:15]\n"
  "s_barrier\n"
  "v_mfma_f32_32x32x16_bf16 v[16:31], v[128:131], v[116:119], v[16:31]\n"
  "ds_read_b128 v[100:103], v144 offset:8192\n"
  "ds_read_b64_tr_b16 v[108:109], v64 offset:16384\n"
  "v_mfma_f32_32x32x16_bf16 v[32:47], v[124:127], v[120:123], v[32:47]\n"
  "ds_read_b64_tr_b16 v[110:111], v64 offset:17408\n"
  "ds_read_b64_tr_b16 v[112:113], v68 offset:16384\n"
  "v_mfma_f32_32x32x16_bf16 v[48:63], v[128:131], v[120:123], v[48:63]\n"
  "ds_read_b64_tr_b16 v[114:115], v68 offset:17408\n"
  "ds_read_b128 v[104:107], v144 offset:28672\n"
  "s_waitcnt lgkmcnt(6)\n"
  "v_mfma_f32_32x32x16_bf16 v[0:15], v[92:95], v[84:87], v[0:15]\n"
  "ds_read_b128 v[116:119], v145 offset:8192\n"
  "ds_read_b64_tr_b16 v[124:125], v65 offset:16384\n"
  "v_mfma_f32_32x32x16_bf16 v[16:31], v[96:99], v[84:87], v[16:31]\n"
  "ds_read_b64_tr_b16 v[126:127], v65 offset:17408\n"
  "ds_read_b64_tr_b16 v[128:129], v69 offset:16384\n"
  "v_mfma_f32_32x32x16_bf16 v[32:47], v[92:95], v[88:91], v[32:47]\n"
  "ds_read_b64_tr_b16 v[130:131], v69 offset:17408\n"
  "ds_read_b128 v[120:123], v145 offset:28672\n"
  "v_mfma_f32_32x32x16_bf16 v[48:63], v[96:99], v[88:91], v[48:63]\n"
  "s_waitcnt lgkmcnt(6)\n"
  "v_mfma_f32_32x32x16_bf16 v[0:15], v[108:111], v[100:103], v[0:15]\n"
  "ds_read_b128 v[84:87], v146 offset:8192\n"
  "ds_read_b64_tr_b16 v[92:93], v66 offset:16384\n"
  "v_mfma_f32_32x32x16_bf16 v[16:31], v[112:115], v[100:103], v[16:31]\n"
  "ds_read_b64_tr_b16 v[94:95], v66 offset:17408\n"
  "ds_read_b64_tr_b16 v[96:97], v70 offset:16384\n"
  "v_mfma_f32_32x32x16_bf16 v[32:47], v[108:111], v[104:107], v[32:47]\n"
  "ds_read_b64_tr_b16 v[98:99], v70 offset:17408\n"
  "ds_read_b128 v[88:91], v146 offset:28672\n"
  "v_mfma_f32_32x32x16_bf16 v[48:63], v[112:115], v[104:107], v[48:63]\n"
  "v_mfma_f32_16x16x32_bf16 v[132:135], v[108:111], v[140:143], v[132:135]\n"
  "v_mfma_f32_16x16x32_bf16 v[136:139], v[112:115], v[140:143], v[136:139]\n"
  "s_waitcnt lgkmcnt(6)\n"
  "v_mfma_f32_32x32x16_bf16 v[0:15], v[124:127], v[116:119], v[0:15]\n"
  "ds_read_b128 v[100:103], v147 offset:8192\n"
  "ds_read_b64_tr_b16 v[108:109], v67 offset:16384\n"
  "v_mfma_f32_32x32x16_bf16 v[16:31], v[128:131], v[116:119], v[16:31]\n"
  "ds_read_b64_tr_b16 v[110:111], v67 offset:17408\n"
  "ds_read_b64_tr_b16 v[112:113], v71 offset:16384\n"
  "v_mfma_f32_32x32x16_bf16 v[32:47], v[124:127], v[120:123], v[32:47]\n"
  "ds_read_b64_tr_b16 v[114:115], v71 offset:17408\n"
  "ds_read_b128 v[104:107], v147 offset:28672\n"
  "v_mfma_f32_32x32x16_bf16 v[48:63], v[128:131], v[120:123], v[48:63]\n"
  "v_mfma_f32_16x16x32_bf16 v[132:135], v[124:127], v[140:143], v[132:135]\n"
  "v_mfma_f32_16x16x32_bf16 v[136:139], v[128:131], v[140:143], v[136:139]\n"
  "s_waitcnt lgkmcnt(6)\n"
  "v_mfma_f32_32x32x16_bf16 v[0:15], v[92:95], v[84:87], v[0:15]\n"
  "s_barrier\n"
  "v_mfma_f32_32x32x16_bf16 v[16:31], v[96:99], v[84:87], v[16:31]\n"
  "ds_read_b128 v[116:119], v144 offset:12288\n"
  "ds_read_b64_tr_b16 v[124:125], v64 offset:32768\n"
  "v_mfma_f32_32x32x16_bf16 v[32:47], v[92:95], v[88:91], v[32:47]\n"
  "ds_read_b64_tr_b16 v[126:127], v64 offset:33792\n"
  "ds_read_b64_tr_b16 v[128:129], v68 offset:32768\n"
  "v_mfma_f32_32x32x16_bf16 v[48:63], v[96:99], v[88:91], v[48:63]\n"
  "ds_read_b64_tr_b16 v[130:131], v68 offset:33792\n"
  "ds_read_b128 v[120:123], v144 offset:32768\n"
  "s_waitcnt lgkmcnt(6)\n"
  "v_mfma_f32_32x32x16_bf16 v[0:15], v[108:111], v[100:103], v[0:15]\n"
  "ds_read_b128 v[84:87], v145 offset:12288\n"
  "ds_read_b64_tr_b16 v[92:93], v65 offset:32768\n"
  "v_mfma_f32_32x32x16_bf16 v[16:31], v[112:115], v[100:103], v[16:31]\n"
  "ds_read_b64_tr_b16 v[94:95], v65 offset:33792\n"
  "ds_read_b64_tr_b16 v[96:97], v69 offset:32768\n"
  "v_mfma_f32_32x32x16_bf16 v[32:47], v[108:111], v[104:107], v[32:47]\n"
  "ds_read_b64_tr_b16 v[98:99], v69 offset:33792\n"
  "ds_read_b128 v[88:91], v145 offset:32768\n"
  "v_mfma_f32_32x32x16_bf16 v[48:63], v[112:115], v[104:107], v[48:63]\n"
  "s_waitcnt lgkmcnt(6)\n"
  "v_mfma_f32_32x32x16_bf16 v[0:15], v[124:127], v[116:119], v[0:15]\n"
  "ds_read_b128 v[100:103], v146 offset:12288\n"
  "ds_read_b64_tr_b16 v[108:109], v66 offset:32768\n"
  "v_mfma_f32_32x32x16_bf16 v[16:31], v[128:131], v[116:119], v[16:31]\n"
  "ds_read_b64_tr_b16 v[110:111], v66 offset:33792\n"
  "ds_read_b64_tr_b16 v[112:113], v70 offset:32768\n"
  "v_mfma_f32_32x32x16_bf16 v[32:47], v[124:127], v[120:123], v[32:47]\n"
  "ds_read_b64_tr_b16 v[114:115], v70 offset:33792\n"
  "ds_read_b128 v[104:107], v146 offset:32768\n"
  "v_mfma_f32_32x32x16_bf16 v[48:63], v[128:131], v[120:123], v[48:63]\n"
  "v_mfma_f32_16x16x32_bf16 v[132:135], v[124:127], v[140:143], v[132:135]\n"
  "v_mfma_f32_16x16x32_bf16 v[136:139], v[128:131], v[140:143], v[136:139]\n"
  "s_waitcnt lgkmcnt(6)\n"
  "v_mfma_f32_32x32x16_bf16 v[0:15], v[92:95], v[84:87], v[0:15]\n"
  "ds_read_b128 v[116:119], v147 offset:12288\n"
  "ds_read_b64_tr_b16 v[124:125], v67 offset:32768\n"
  "v_mfma_f32_32x32x16_bf16 v[16:31], v[96:99], v[84:87], v[16:31]\n"
  "ds_read_b64_tr_b16 v[126:127], v67 offset:33792\n"
  "ds_read_b64_tr_b16 v[128:129], v71 offset:32768\n"
  "v_mfma_f32_32x32x16_bf16 v[32:47], v[92:95], v[88:91], v[32:47]\n"
  "ds_read_b64_tr_b16 v[130:131], v71 offset:33792\n"
  "ds_read_b128 v[120:123], v147 offset:32768\n"
  "v_mfma_f32_32x32x16_bf16 v[48:63], v[96:99], v[88:91], v[48:63]\n"
  "v_mfma_f32_16x16x32_bf16 v[132:135], v[92:95], v[140:143], v[132:135]\n"
  "v_mfma_f32_16x16x32_bf16 v[136:139], v[96:99], v[140:143], v[136:139]\n"
  "s_waitcnt lgkmcnt(6)\n"
  "v_mfma_f32_32x32x16_bf16 v[0:15], v[108:111], v[100:103], v[0:15]\n"
  "s_barrier\n"
  "v_mfma_f32_32x32x16_bf16 v[16:31], v[112:115], v[100:103], v[16:31]\n"
  "ds_read_b128 v[84:87], v144 offset:16384\n"
  "ds_read_b64_tr_b16 v[92:93], v64 offset:49152\n"
  "v_mfma_f32_32x32x16_bf16 v[32:47], v[108:111], v[104:107], v[32:47]\n"
  "ds_read_b64_tr_b16 v[94:95], v64 offset:50176\n"
  "ds_read_b64_tr_b16 v[96:97], v68 offset:49152\n"
  "v_mfma_f32_32x32x16_bf16 v[48:63], v[112:115], v[104:107], v[48:63]\n"
  "ds_read_b64_tr_b16 v[98:99], v68 offset:50176\n"
  "ds_read_b128 v[88:91], v144 offset:36864\n"
  "s_waitcnt lgkmcnt(6)\n"
  "v_mfma_f32_32x32x16_bf16 v[0:15], v[124:127], v[116:119], v[0:15]\n"
  "ds_read_b128 v[100:103], v145 offset:16384\n"
  "ds_read_b64_tr_b16 v[108:109], v65 offset:49152\n"
  "v_mfma_f32_32x32x16_bf16 v[16:31], v[128:131], v[116:119], v[16:31]\n"
  "ds_read_b64_tr_b16 v[110:111], v65 offset:50176\n"
  "ds_read_b64_tr_b16 v[112:113], v69 offset:49152\n"
  "v_mfma_f32_32x32x16_bf16 v[32:47], v[124:127], v[120:123], v[32:47]\n"
  "ds_read_b64_tr_b16 v[114:115], v69 offset:50176\n"
  "ds_read_b128 v[104:107], v145 offset:36864\n"
  "v_mfma_f32_32x32x16_bf16 v[48:63], v[128:131], v[120:123], v[48:63]\n"
  "s_waitcnt lgkmcnt(6)\n"
  "v_mfma_f32_32x32x16_bf16 v[0:15], v[92:95], v[84:87], v[0:15]\n"
  "ds_read_b128 v[116:119], v146 offset:16384\n"
  "ds_read_b64_tr_b16 v[124:125], v66 offset:49152\n"
  "v_mfma_f32_32x32x16_bf16 v[16:31], v[96:99], v[84:87], v[16:31]\n"
  "ds_read_b64_tr_b16 v[126:127], v66 offset:50176\n"
  "ds_read_b64_tr_b16 v[128:129], v70 offset:49152\n"
  "v_mfma_f32_32x32x16_bf16 v[32:47], v[92:95], v[88:91], v[32:47]\n"
  "ds_read_b64_tr_b16 v[130:131], v70 offset:50176\n"
  "ds_read_b128 v[120:123], v146 offset:36864\n"
  "v_mfma_f32_32x32x16_bf16 v[48:63], v[96:99], v[88:91], v[48:63]\n"
  "v_mfma_f32_16x16x32_bf16 v[132:135], v[92:95], v[140:143], v[132:135]\n"
  "v_mfma_f32_16x16x32_bf16 v[136:139], v[96:99], v[140:143], v[136:139]\n"
  "s_waitcnt lgkmcnt(6)\n"
  "v_mfma_f32_16x16x32_bf16 v[132:135], v[108:111], v[140:143], v[132:135]\n"
  "v_mfma_f32_16x16x32_bf16 v[136:139], v[112:115], v[140:143], v[136:139]\n"
  "v_mfma_f32_32x32x16_bf16 v[0:15], v[108:111], v[100:103], v[0:15]\n"
  "ds_read_b128 v[84:87], v147 offset:16384\n"
  "ds_read_b64_tr_b16 v[92:93], v67 offset:49152\n"
  "v_mfma_f32_32x32x16_bf16 v[16:31], v[112:115], v[100:103], v[16:31]\n"
  "ds_read_b64_tr_b16 v[94:95], v67 offset:50176\n"
  "ds_read_b64_tr_b16 v[96:97], v71 offset:49152\n"
  "v_mfma_f32_32x32x16_bf16 v[32:47], v[108:111], v[104:107], v[32:47]\n"
  "ds_read_b64_tr_b16 v[98:99], v71 offset:50176\n"
  "ds_read_b128 v[88:91], v147 offset:36864\n"
  "s_mov_b32 exec_lo, 0x30003\n"
  "s_mov_b32 exec_hi, 0x30003\n"
  "ds_write_b128 v148, v[132:135] offset:0\n"
  "ds_write_b128 v148, v[136:139] offset:512\n"
  "s_mov_b64 exec, -1\n"
  "v_mfma_f32_32x32x16_bf16 v[48:63], v[112:115], v[104:107], v[48:63]\n"
  "s_waitcnt lgkmcnt(8)\n"
  "v_mfma_f32_32x32x16_bf16 v[0:15], v[124:127], v[116:119], v[0:15]\n"
  "s_waitcnt lgkmcnt(0)\n"
  "s_barrier\n"
  "ds_read_b128 v[158:161], v150 offset:0\n"
  "ds_read_b128 v[162:165], v150 offset:512\n"
  "ds_read_b32 v134, v149 offset:0\n"
  "ds_read_b32 v138, v149 offset:512\n"
  "v_mfma_f32_32x32x16_bf16 v[16:31], v[128:131], v[116:119], v[16:31]\n"
  "v_mfma_f32_32x32x16_bf16 v[32:47], v[124:127], v[120:123], v[32:47]\n"
  "v_mfma_f32_32x32x16_bf16 v[48:63], v[128:131], v[120:123], v[48:63]\n"
  "s_waitcnt lgkmcnt(0)\n"
  "200:\n"
  "v_cmp_ne_u32 vcc, s46, v159\n"
  "v_cmp_ne_u32 s[40:41], s47, v161\n"
  "s_or_b64 vcc, vcc, s[40:41]\n"
  "s_cbranch_vccnz 300f\n"
  "400:\n"
  "v_cvt_pk_bf16_f32 v72, -v158, -v158\n"
  "v_and_b32 v72, 0xffff0000, v72\n"
  "v_sub_f32 v72, -v158, v72\n"
  "v_cvt_pk_bf16_f32 v72, -v158, v72\n"
  "v_mfma_f32_32x32x16_bf16 v[0:15], v[92:95], v[84:87], v[0:15]\n"
  "v_mov_b32 v73, v72\n"
  "201:\n"
  "v_cmp_ne_u32 vcc, s46, v163\n"
  "v_cmp_ne_u32 s[40:41], s47, v165\n"
  "s_or_b64 vcc, vcc, s[40:41]\n"
  "s_cbranch_vccnz 301f\n"
  "401:\n"
  "v_cvt_pk_bf16_f32 v82, -v162, -v162\n"
  "v_and_b32 v82, 0xffff0000, v82\n"
  "v_sub_f32 v82, -v162, v82\n"
  "v_mfma_f32_32x32x16_bf16 v[16:31], v[96:99], v[84:87], v[16:31]\n"
  "v_cvt_pk_bf16_f32 v82, -v162, v82\n"
  "v_mov_b32 v83, v82\n"
  "v_cvt_pk_bf16_f32 v132, v134, v134\n"
  "v_and_b32 v135, 0xffff0000, v132\n"
  "v_sub_f32 v134, v134, v135\n"
  "v_mfma_f32_32x32x16_bf16 v[32:47], v[92:95], v[88:91], v[32:47]\n"
  "v_cvt_pk_bf16_f32 v133, v134, v134\n"
  "v_cvt_pk_bf16_f32 v136, v138, v138\n"
  "v_and_b32 v139, 0xffff0000, v136\n"
  "v_sub_f32 v138, v138, v139\n"
  "v_cvt_pk_bf16_f32 v137, v138, v138\n"
  "v_mfma_f32_32x32x16_bf16 v[48:63], v[96:99], v[88:91], v[48:63]\n"
  "s_nop 1\n"
  "v_mfma_f32_32x32x8_bf16 v[0:15], v[132:133], v[72:73], v[0:15]\n"
  "v_mfma_f32_32x32x8_bf16 v[16:31], v[136:137], v[72:73], v[16:31]\n"
  "v_mfma_f32_32x32x8_bf16 v[32:47], v[132:133], v[82:83], v[32:47]\n"
  "v_mfma_f32_32x32x8_bf16 v[48:63], v[136:137], v[82:83], v[48:63]\n"
  "v_mul_f32 v159, s62, v160\n"
  "v_mul_f32 v163, s62, v164\n"
  "v_and_b32 v92, 0x1f0, v156\n"
  "v_bfe_i32 v93, v156, 9, 1\n"
  "v_and_b32 v94, s55, v93\n"
  "v_xor_b32 v95, s54, v92\n"
  "v_xor_b32 v84, v94, v95\n"
  "v_and_b32 v94, s57, v93\n"
  "v_xor_b32 v95, s56, v92\n"
  "v_xor_b32 v85, v94, v95\n"
  "v_and_b32 v94, s59, v93\n"
  "v_xor_b32 v95, s58, v92\n"
  "v_xor_b32 v86, v94, v95\n"
  "v_and_b32 v94, s61, v93\n"
  "v_xor_b32 v95, s60, v92\n"
  "v_xor_b32 v87, v94, v95\n"
  "s_add_u32 s40, s56, 1024\n"
  "v_and_b32 v94, s68, v93\n"
  "v_xor_b32 v95, s40, v92\n"
  "v_xor_b32 v88, v94, v95\n"
  "s_add_u32 s40, s60, 1024\n"
  "v_and_b32 v94, s69, v93\n"
  "v_xor_b32 v95, s40, v92\n"
  "v_xor_b32 v89, v94, v95\n"
  "s_barrier\n"
  "v_mul_f32 v0, v159, v0\n"
  "v_mul_f32 v1, v159, v1\n"
  "v_mul_f32 v2, v159, v2\n"
  "v_mul_f32 v3, v159, v3\n"
  "v_mul_f32 v4, v159, v4\n"
  "v_mul_f32 v5, v159, v5\n"
  "v_mul_f32 v6, v159, v6\n"
  "v_mul_f32 v7, v159, v7\n"
  "v_cvt_pk_bf16_f32 v0, v0, v1\n"
  "v_cvt_pk_bf16_f32 v1, v2, v3\n"
  "v_cvt_pk_bf16_f32 v2, v4, v5\n"
  "v_cvt_pk_bf16_f32 v3, v6, v7\n"
  "buffer_store_dwordx4 v[0:3], v84, s[16:19], 0 offen sc1\n"
  "v_mul_f32 v8, v159, v8\n"
  "v_mul_f32 v9, v159, v9\n"
  "v_mul_f32 v10, v159, v10\n"
  "v_mul_f32 v11, v159, v11\n"
  "v_mul_f32 v12, v159, v12\n"
  "v_mul_f32 v13, v159, v13\n"
  "v_mul_f32 v14, v159, v14\n"
  "v_mul_f32 v15, v159, v15\n"
  "v_cvt_pk_bf16_f32 v8, v8, v9\n"
  "v_cvt_pk_bf16_f32 v9, v10, v11\n"
  "v_cvt_pk_bf16_f32 v10, v12, v13\n"
  "v_cvt_pk_bf16_f32 v11, v14, v15\n"
  "buffer_store_dwordx4 v[8:11], v85, s[16:19], 0 offen sc1\n"
  "v_mul_f32 v16, v159, v16\n"
  "v_mul_f32 v17, v159, v17\n"
  "v_mul_f32 v18, v159, v18\n"
  "v_mul_f32 v19, v159, v19\n"
  "v_mul_f32 v20, v159, v20\n"
  "v_mul_f32 v21, v159, v21\n"
  "v_mul_f32 v22, v159, v22\n"
  "v_mul_f32 v23, v159, v23\n"
  "v_cvt_pk_bf16_f32 v16, v16, v17\n"
  "v_cvt_pk_bf16_f32 v17, v18, v19\n"
  "v_cvt_pk_bf16_f32 v18, v20, v21\n"
  "v_cvt_pk_bf16_f32 v19, v22, v23\n"
  "buffer_store_dwordx4 v[16:19], v86, s[16:19], 0 offen sc1\n"
  "s_barrier\n"
  "v_mul_f32 v24, v159, v24\n"
  "v_mul_f32 v25, v159, v25\n"
  "v_mul_f32 v26, v159, v26\n"
  "v_mul_f32 v27, v159, v27\n"
  "v_mul_f32 v28, v159, v28\n"
  "v_mul_f32 v29, v159, v29\n"
  "v_mul_f32 v30, v159, v30\n"
  "v_mul_f32 v31, v159, v31\n"
  "v_cvt_pk_bf16_f32 v24, v24, v25\n"
  "v_cvt_pk_bf16_f32 v25, v26, v27\n"
  "v_cvt_pk_bf16_f32 v26, v28, v29\n"
  "v_cvt_pk_bf16_f32 v27, v30, v31\n"
  "buffer_store_dwordx4 v[24:27], v87, s[16:19], 0 offen sc1\n"
  "v_mul_f32 v32, v163, v32\n"
  "v_mul_f32 v33, v163, v33\n"
  "v_mul_f32 v34, v163, v34\n"
  "v_mul_f32 v35, v163, v35\n"
  "v_mul_f32 v36, v163, v36\n"
  "v_mul_f32 v37, v163, v37\n"
  "v_mul_f32 v38, v163, v38\n"
  "v_mul_f32 v39, v163, v39\n"
  "v_cvt_pk_bf16_f32 v32, v32, v33\n"
  "v_cvt_pk_bf16_f32 v33, v34, v35\n"
  "v_cvt_pk_bf16_f32 v34, v36, v37\n"
  "v_cvt_pk_bf16_f32 v35, v38, v39\n"
  "buffer_store_dwordx4 v[32:35], v84, s[16:19], 0 offen offset:1024 sc1\n"
  "v_mul_f32 v40, v163, v40\n"
  "v_mul_f32 v41, v163, v41\n"
  "v_mul_f32 v42, v163, v42\n"
  "v_mul_f32 v43, v163, v43\n"
  "v_mul_f32 v44, v163, v44\n"
  "v_mul_f32 v45, v163, v45\n"
  "v_mul_f32 v46, v163, v46\n"
  "v_mul_f32 v47, v163, v47\n"
  "v_cvt_pk_bf16_f32 v40, v40, v41\n"
  "v_cvt_pk_bf16_f32 v41, v42, v43\n"
  "v_cvt_pk_bf16_f32 v42, v44, v45\n"
  "v_cvt_pk_bf16_f32 v43, v46, v47\n"
  "buffer_store_dwordx4 v[40:43], v88, s[16:19], 0 offen sc1\n"
  "s_barrier\n"
  "v_mul_f32 v48, v163, v48\n"
  "v_mul_f32 v49, v163, v49\n"
  "v_mul_f32 v50, v163, v50\n"
  "v_mul_f32 v51, v163, v51\n"
  "v_mul_f32 v52, v163, v52\n"
  "v_mul_f32 v53, v163, v53\n"
  "v_mul_f32 v54, v163, v54\n"
  "v_mul_f32 v55, v163, v55\n"
  "v_cvt_pk_bf16_f32 v48, v48, v49\n"
  "v_cvt_pk_bf16_f32 v49, v50, v51\n"
  "v_cvt_pk_bf16_f32 v50, v52, v53\n"
  "v_cvt_pk_bf16_f32 v51, v54, v55\n"
  "buffer_store_dwordx4 v[48:51], v86, s[16:19], 0 offen offset:1024 sc1\n"
  "v_mul_f32 v56, v163, v56\n"
  "v_mul_f32 v57, v163, v57\n"
  "v_mul_f32 v58, v163, v58\n"
  "v_mul_f32 v59, v163, v59\n"
  "v_mul_f32 v60, v163, v60\n"
  "v_mul_f32 v61, v163, v61\n"
  "v_mul_f32 v62, v163, v62\n"
  "v_mul_f32 v63, v163, v63\n"
  "v_cvt_pk_bf16_f32 v56, v56, v57\n"
  "v_cvt_pk_bf16_f32 v57, v58, v59\n"
  "v_cvt_pk_bf16_f32 v58, v60, v61\n"
  "v_cvt_pk_bf16_f32 v59, v62, v63\n"
  "buffer_store_dwordx4 v[56:59], v89, s[16:19], 0 offen sc1\n"
  "s_barrier\n"
  "v_mov_b32 v0, 135168\n"
  "v_bfe_u32 v1, v156, 5, 1\n"
  "v_lshlrev_b32 v2, 2, v1\n"
  "v_cndmask_b32 v2, v2, 8, s[38:39]\n"
  "v_xor_b32 v2, v2, v152\n"
  "v_bfe_u32 v3, v156, 4, 1\n"
  "v_lshlrev_b32 v3, 3, v3\n"
  "v_lshl_or_b32 v3, v1, 9, v3\n"
  "s_add_u32 s40, s76, 2048\n"
  "v_mov_b32 v4, s40\n"
  "v_cndmask_b32 v3, v3, v4, s[38:39]\n"
  "v_lshl_add_u32 v2, v2, 4, v3\n"
  "s_add_u32 s40, s77, 6144\n"
  "v_add_u32 v2, s40, v2\n"
  "v_add_u32 v3, 0x400, v2\n"
  "v_add_u32 v4, 0x200, v2\n"
  "v_cndmask_b32 v3, v3, v4, s[38:39]\n"
  "v_xor_b32 v4, 64, v2\n"
  "v_cndmask_b32 v64, v2, v0, s[48:49]\n"
  "v_cndmask_b32 v65, v4, v0, s[48:49]\n"
  "v_add_u32 v2, 0x100, v2\n"
  "v_add_u32 v4, 0x100, v4\n"
  "v_cndmask_b32 v66, v2, v0, s[48:49]\n"
  "v_cndmask_b32 v67, v4, v0, s[48:49]\n"
  "v_xor_b32 v4, 64, v3\n"
  "v_cndmask_b32 v68, v3, v0, s[48:49]\n"
  "v_cndmask_b32 v69, v4, v0, s[48:49]\n"
  "v_add_u32 v3, 0x100, v3\n"
  "v_add_u32 v4, 0x100, v4\n"
  "v_cndmask_b32 v70, v3, v0, s[48:49]\n"
  "v_cndmask_b32 v71, v4, v0, s[48:49]\n"
  "v_and_b32 v1, 0x1f0, v156\n"
  "v_add_u32 v1, s76, v1\n"
  "v_add_u32 v158, s77, v156\n"
  "v_add_u32 v72, s78, v156\n"
  "v_add_u32 v82, s80, v156\n"
  "v_add_u32 v165, s82, v156\n"
  "s_waitcnt vmcnt(0)\n"
  "s_barrier\n"
  "v_add_u32 v161, s83, v156\n"
  "v_add_u32 v162, s84, v156\n"
  "v_lshrrev_b32 v163, 2, v156\n"
  "v_add_u32 v163, s85, v163\n"
  "s_add_u32 s40, s77, 2048\n"
  "v_add_u32 v2, s40, v1\n"
  "v_cndmask_b32 v159, v2, v0, s[36:37]\n"
  "s_add_u32 s40, s77, 2560\n"
  "v_add_u32 v2, s40, v1\n"
  "v_cndmask_b32 v160, v2, v0, s[36:37]\n"
  "s_add_u32 s40, s79, 0\n"
  "v_add_u32 v2, s40, v1\n"
  "v_cndmask_b32 v73, v2, v0, s[36:37]\n"
  "s_add_u32 s40, s81, 0\n"
  "v_add_u32 v2, s40, v1\n"
  "v_cndmask_b32 v83, v2, v0, s[36:37]\n"
  "v_add_u32 v2, 5120, v1\n"
  "v_cndmask_b32 v164, v2, v0, s[36:37]\n"
  "s_add_u32 s40, s82, 1024\n"
  "v_add_u32 v2, s40, v1\n"
  "v_cndmask_b32 v166, v2, v0, s[36:37]\n"
  "v_mov_b32 v157, 0xdddd\n"
  "v_mov_b64 v[0:1], 0\n"
  "v_mov_b64 v[2:3], 0\n"
  "v_mov_b64 v[4:5], 0\n"
  "v_mov_b64 v[6:7], 0\n"
  "v_mov_b64 v[8:9], 0\n"
  "v_mov_b64 v[10:11], 0\n"
  "v_mov_b64 v[12:13], 0\n"
  "v_mov_b64 v[14:15], 0\n"
  "v_mov_b64 v[16:17], 0\n"
  "v_mov_b64 v[18:19], 0\n"
  "v_mov_b64 v[20:21], 0\n"
  "v_mov_b64 v[22:23], 0\n"
  "v_mov_b64 v[24:25], 0\n"
  "v_mov_b64 v[26:27], 0\n"
  "v_mov_b64 v[28:29], 0\n"
  "v_mov_b64 v[30:31], 0\n"
  "s_barrier\n"
  "s_barrier\n"
  "s_barrier\n"
  "s_barrier\n"
  "s_barrier\n"
  "s_branch 9f\n"
  "300:\n"
  "s_add_u32 s44, s44, 1\n"
  "s_cmp_gt_u32 s44, 524288\n"
  "s_cbranch_scc1 400b\n"
  "buffer_load_dwordx4 v[158:161], v150, s[16:19], s64 offen offset:0 sc1\n"
  "s_waitcnt vmcnt(0)\n"
  "s_branch 200b\n"
  "301:\n"
  "s_add_u32 s44, s44, 1\n"
  "s_cmp_gt_u32 s44, 524288\n"
  "s_cbranch_scc1 401b\n"
  "buffer_load_dwordx4 v[162:165], v150, s[16:19], s64 offen offset:512 sc1\n"
  "s_waitcnt vmcnt(0)\n"
  "s_branch 201b\n"
  "1:\n"
  "s_setprio 1\n"
  "s_barrier\n"
  "v_mul_u32_u24 v0, 0xc0, v152\n"
  "v_add_u32 v0, v0, v153\n"
  "s_xor_b32 s40, s4, 0\n"
  "s_mul_i32 s40, s40, 3072\n"
  "s_add_u32 s40, s40, 86016\n"
  "v_add_u32 v116, s40, v0\n"
  "s_xor_b32 s40, s4, 1\n"
  "s_mul_i32 s40, s40, 3072\n"
  "s_add_u32 s40, s40, 86016\n"
  "v_add_u32 v117, s40, v0\n"
  "s_xor_b32 s40, s4, 2\n"
  "s_mul_i32 s40, s40, 3072\n"
  "s_add_u32 s40, s40, 86016\n"
  "v_add_u32 v118, s40, v0\n"
  "s_xor_b32 s40, s4, 3\n"
  "s_mul_i32 s40, s40, 3072\n"
  "s_add_u32 s40, s40, 86016\n"
  "v_add_u32 v119, s40, v0\n"
  "v_lshrrev_b32 v0, 2, v156\n"
  "v_and_b32 v1, 0x80, v0\n"
  "s_barrier\n"
  "v_add3_u32 v149, v0, v1, s72\n"
  "v_mov_b32 v0, 135168\n"
  "v_bfe_u32 v1, v156, 5, 1\n"
  "v_lshlrev_b32 v2, 2, v1\n"
  "v_cndmask_b32 v2, v2, 8, s[38:39]\n"
  "v_xor_b32 v2, v2, v152\n"
  "v_bfe_u32 v3, v156, 4, 1\n"
  "v_lshlrev_b32 v3, 3, v3\n"
  "v_lshl_or_b32 v3, v1, 9, v3\n"
  "s_add_u32 s40, s76, 2048\n"
  "v_mov_b32 v4, s40\n"
  "v_cndmask_b32 v3, v3, v4, s[38:39]\n"
  "v_lshl_add_u32 v2, v2, 4, v3\n"
  "s_add_u32 s40, s77, 6144\n"
  "v_add_u32 v2, s40, v2\n"
  "v_add_u32 v3, 0x400, v2\n"
  "v_add_u32 v4, 0x200, v2\n"
  "v_cndmask_b32 v3, v3, v4, s[38:39]\n"
  "v_xor_b32 v4, 64, v2\n"
  "v_cndmask_b32 v64, v2, v0, s[48:49]\n"
  "s_barrier\n"
  "v_cndmask_b32 v65, v4, v0, s[48:49]\n"
  "v_add_u32 v2, 0x100, v2\n"
  "v_add_u32 v4, 0x100, v4\n"
  "v_cndmask_b32 v66, v2, v0, s[48:49]\n"
  "v_cndmask_b32 v67, v4, v0, s[48:49]\n"
  "v_xor_b32 v4, 64, v3\n"
  "v_cndmask_b32 v68, v3, v0, s[48:49]\n"
  "v_cndmask_b32 v69, v4, v0, s[48:49]\n"
  "v_add_u32 v3, 0x100, v3\n"
  "v_add_u32 v4, 0x100, v4\n"
  "v_cndmask_b32 v70, v3, v0, s[48:49]\n"
  "v_cndmask_b32 v71, v4, v0, s[48:49]\n"
  "v_and_b32 v1, 0x1f0, v156\n"
  "v_add_u32 v1, s76, v1\n"
  "v_add_u32 v158, s77, v156\n"
  "v_add_u32 v72, s78, v156\n"
  "v_add_u32 v82, s80, v156\n"
  "v_add_u32 v165, s82, v156\n"
  "v_add_u32 v161, s83, v156\n"
  "v_add_u32 v162, s84, v156\n"
  "s_barrier\n"
  "v_lshrrev_b32 v163, 2, v156\n"
  "v_add_u32 v163, s85, v163\n"
  "s_add_u32 s40, s77, 2048\n"
  "v_add_u32 v2, s40, v1\n"
  "v_cndmask_b32 v159, v2, v0, s[36:37]\n"
  "s_add_u32 s40, s77, 2560\n"
  "v_add_u32 v2, s40, v1\n"
  "v_cndmask_b32 v160, v2, v0, s[36:37]\n"
  "s_add_u32 s40, s79, 0\n"
  "v_add_u32 v2, s40, v1\n"
  "v_cndmask_b32 v73, v2, v0, s[36:37]\n"
  "s_add_u32 s40, s81, 0\n"
  "v_add_u32 v2, s40, v1\n"
  "v_cndmask_b32 v83, v2, v0, s[36:37]\n"
  "v_add_u32 v2, 5120, v1\n"
  "v_cndmask_b32 v164, v2, v0, s[36:37]\n"
  "s_add_u32 s40, s82, 1024\n"
  "v_add_u32 v2, s40, v1\n"
  "v_cndmask_b32 v166, v2, v0, s[36:37]\n"
  "v_mov_b32 v157, 0xdddd\n"
  "s_barrier\n"
  "s_setprio 0\n"
  "s_barrier\n"
  "s_barrier\n"
  "s_barrier\n"
  "s_barrier\n"
  "s_barrier\n"
  "s_barrier\n"
  "ds_read_b128 v[12:15], v150\n"
  "ds_read_b128 v[100:103], v144 offset:0\n"
  "ds_read_b64_tr_b16 v[104:105], v116 offset:61440\n"
  "ds_read_b64_tr_b16 v[106:107], v116 offset:62208\n"
  "ds_read_b64_tr_b16 v[108:109], v116 offset:61504\n"
  "ds_read_b64_tr_b16 v[110:111], v116 offset:62272\n"
  "ds_read_b64_tr_b16 v[112:113], v116 offset:61568\n"
  "ds_read_b64_tr_b16 v[114:115], v116 offset:62336\n"
  "ds_read_b128 v[16:19], v145 offset:0\n"
  "ds_read_b64_tr_b16 v[20:21], v117 offset:61440\n"
  "ds_read_b64_tr_b16 v[22:23], v117 offset:62208\n"
  "ds_read_b64_tr_b16 v[24:25], v117 offset:61504\n"
  "ds_read_b64_tr_b16 v[26:27], v117 offset:62272\n"
  "ds_read_b64_tr_b16 v[28:29], v117 offset:61568\n"
  "ds_read_b64_tr_b16 v[30:31], v117 offset:62336\n"
  "s_waitcnt lgkmcnt(7)\n"
  "v_mfma_f32_32x32x16_bf16 v[32:47], v[104:107], v[100:103], 0\n"
  "ds_read_b128 v[84:87], v146 offset:0\n"
  "ds_read_b64_tr_b16 v[88:89], v118 offset:61440\n"
  "ds_read_b64_tr_b16 v[90:91], v118 offset:62208\n"
  "210:\n"
  "v_cmp_ne_u32 vcc, s46, v13\n"
  "v_cmp_ne_u32 s[40:41], s47, v15\n"
  "s_or_b64 vcc, vcc, s[40:41]\n"
  "s_cbranch_vccnz 310f\n"
  "410:\n"
  "v_cvt_pk_bf16_f32 v136, -v12, -v12\n"
  "v_mfma_f32_32x32x16_bf16 v[48:63], v[108:111], v[100:103], 0\n"
  "ds_read_b64_tr_b16 v[92:93], v118 offset:61504\n"
  "ds_read_b64_tr_b16 v[94:95], v118 offset:62272\n"
  "v_and_b32 v136, 0xffff0000, v136\n"
  "v_sub_f32 v136, -v12, v136\n"
  "v_mfma_f32_32x32x16_bf16 v[120:135], v[112:115], v[100:103], 0\n"
  "ds_read_b64_tr_b16 v[96:97], v118 offset:61568\n"
  "ds_read_b64_tr_b16 v[98:99], v118 offset:62336\n"
  "v_cvt_pk_bf16_f32 v136, -v12, v136\n"
  "v_mov_b32 v137, v136\n"
  "v_mfma_f32_16x16x32_bf16 v[0:3], v[104:107], v[140:143], 0\n"
  "v_mfma_f32_16x16x32_bf16 v[4:7], v[108:111], v[140:143], 0\n"
  "v_mfma_f32_16x16x32_bf16 v[8:11], v[112:115], v[140:143], 0\n"
  "s_waitcnt lgkmcnt(7)\n"
  "v_mfma_f32_32x32x16_bf16 v[32:47], v[20:23], v[16:19], v[32:47]\n"
  "ds_read_b128 v[100:103], v147 offset:0\n"
  "ds_read_b64_tr_b16 v[104:105], v119 offset:61440\n"
  "ds_read_b64_tr_b16 v[106:107], v119 offset:62208\n"
  "v_mfma_f32_32x32x16_bf16 v[48:63], v[24:27], v[16:19], v[48:63]\n"
  "ds_read_b64_tr_b16 v[108:109], v119 offset:61504\n"
  "ds_read_b64_tr_b16 v[110:111], v119 offset:62272\n"
  "v_mfma_f32_32x32x16_bf16 v[120:135], v[28:31], v[16:19], v[120:135]\n"
  "ds_read_b64_tr_b16 v[112:113], v119 offset:61568\n"
  "ds_read_b64_tr_b16 v[114:115], v119 offset:62336\n"
  "s_waitcnt lgkmcnt(7)\n"
  "v_mfma_f32_32x32x16_bf16 v[32:47], v[88:91], v[84:87], v[32:47]\n"
  "s_waitcnt lgkmcnt(0)\n"
  "s_barrier\n"
  "v_mfma_f32_32x32x16_bf16 v[48:63], v[92:95], v[84:87], v[48:63]\n"
  "ds_read_b128 v[16:19], v144 offset:4096\n"
  "ds_read_b64_tr_b16 v[20:21], v116 offset:0\n"
  "ds_read_b64_tr_b16 v[22:23], v116 offset:768\n"
  "ds_read_b64_tr_b16 v[24:25], v116 offset:64\n"
  "v_mfma_f32_32x32x16_bf16 v[120:135], v[96:99], v[84:87], v[120:135]\n"
  "ds_read_b64_tr_b16 v[26:27], v116 offset:832\n"
  "ds_read_b64_tr_b16 v[28:29], v116 offset:128\n"
  "ds_read_b64_tr_b16 v[30:31], v116 offset:896\n"
  "v_mfma_f32_32x32x16_bf16 v[32:47], v[104:107], v[100:103], v[32:47]\n"
  "ds_read_b128 v[84:87], v145 offset:4096\n"
  "ds_read_b64_tr_b16 v[88:89], v117 offset:0\n"
  "ds_read_b64_tr_b16 v[90:91], v117 offset:768\n"
  "v_mfma_f32_32x32x16_bf16 v[48:63], v[108:111], v[100:103], v[48:63]\n"
  "ds_read_b64_tr_b16 v[92:93], v117 offset:64\n"
  "ds_read_b64_tr_b16 v[94:95], v117 offset:832\n"
  "v_mfma_f32_32x32x16_bf16 v[120:135], v[112:115], v[100:103], v[120:135]\n"
  "ds_read_b64_tr_b16 v[96:97], v117 offset:128\n"
  "ds_read_b64_tr_b16 v[98:99], v117 offset:896\n"
  "s_waitcnt lgkmcnt(7)\n"
  "v_mfma_f32_32x32x16_bf16 v[32:47], v[20:23], v[16:19], v[32:47]\n"
  "ds_read_b128 v[100:103], v146 offset:4096\n"
  "ds_read_b64_tr_b16 v[104:105], v118 offset:0\n"
  "ds_read_b64_tr_b16 v[106:107], v118 offset:768\n"
  "v_mfma_f32_32x32x16_bf16 v[48:63], v[24:27], v[16:19], v[48:63]\n"
  "ds_read_b64_tr_b16 v[108:109], v118 offset:64\n"
  "ds_read_b64_tr_b16 v[110:111], v118 offset:832\n"
  "v_mfma_f32_32x32x16_bf16 v[120:135], v[28:31], v[16:19], v[120:135]\n"
  "ds_read_b64_tr_b16 v[112:113], v118 offset:128\n"
  "ds_read_b64_tr_b16 v[114:115], v118 offset:896\n"
  "v_mfma_f32_16x16x32_bf16 v[0:3], v[20:23], v[140:143], v[0:3]\n"
  "v_mfma_f32_16x16x32_bf16 v[4:7], v[24:27], v[140:143], v[4:7]\n"
  "v_mfma_f32_16x16x32_bf16 v[8:11], v[28:31], v[140:143], v[8:11]\n"
  "s_waitcnt lgkmcnt(7)\n"
  "v_mfma_f32_32x32x16_bf16 v[32:47], v[88:91], v[84:87], v[32:47]\n"
  "ds_read_b128 v[16:19], v147 offset:4096\n"
  "ds_read_b64_tr_b16 v[20:21], v119 offset:0\n"
  "ds_read_b64_tr_b16 v[22:23], v119 offset:768\n"
  "v_mfma_f32_32x32x16_bf16 v[48:63], v[92:95], v[84:87], v[48:63]\n"
  "ds_read_b64_tr_b16 v[24:25], v119 offset:64\n"
  "ds_read_b64_tr_b16 v[26:27], v119 offset:832\n"
  "v_mfma_f32_32x32x16_bf16 v[120:135], v[96:99], v[84:87], v[120:135]\n"
  "ds_read_b64_tr_b16 v[28:29], v119 offset:128\n"
  "ds_read_b64_tr_b16 v[30:31], v119 offset:896\n"
  "s_waitcnt lgkmcnt(7)\n"
  "v_mfma_f32_32x32x16_bf16 v[32:47], v[104:107], v[100:103], v[32:47]\n"
  "s_barrier\n"
  "v_mfma_f32_32x32x16_bf16 v[48:63], v[108:111], v[100:103], v[48:63]\n"
  "ds_read_b128 v[84:87], v144 offset:8192\n"
  "ds_read_b64_tr_b16 v[88:89], v116 offset:12288\n"
  "ds_read_b64_tr_b16 v[90:91], v116 offset:13056\n"
  "ds_read_b64_tr_b16 v[92:93], v116 offset:12352\n"
  "v_mfma_f32_32x32x16_bf16 v[120:135], v[112:115], v[100:103], v[120:135]\n"
  "ds_read_b64_tr_b16 v[94:95], v116 offset:13120\n"
  "ds_read_b64_tr_b16 v[96:97], v116 offset:12416\n"
  "ds_read_b64_tr_b16 v[98:99], v116 offset:13184\n"
  "s_waitcnt lgkmcnt(7)\n"
  "v_mfma_f32_32x32x16_bf16 v[32:47], v[20:23], v[16:19], v[32:47]\n"
  "ds_read_b128 v[100:103], v145 offset:8192\n"
  "ds_read_b64_tr_b16 v[104:105], v117 offset:12288\n"
  "ds_read_b64_tr_b16 v[106:107], v117 offset:13056\n"
  "v_mfma_f32_32x32x16_bf16 v[48:63], v[24:27], v[16:19], v[48:63]\n"
  "ds_read_b64_tr_b16 v[108:109], v117 offset:12352\n"
  "ds_read_b64_tr_b16 v[110:111], v117 offset:13120\n"
  "v_mfma_f32_32x32x16_bf16 v[120:135], v[28:31], v[16:19], v[120:135]\n"
  "ds_read_b64_tr_b16 v[112:113], v117 offset:12416\n"
  "ds_read_b64_tr_b16 v[114:115], v117 offset:13184\n"
  "s_waitcnt lgkmcnt(7)\n"
  "v_mfma_f32_32x32x16_bf16 v[32:47], v[88:91], v[84:87], v[32:47]\n"
  "ds_read_b128 v[16:19], v146 offset:8192\n"
  "ds_read_b64_tr_b16 v[20:21], v118 offset:12288\n"
  "ds_read_b64_tr_b16 v[22:23], v118 offset:13056\n"
  "v_mfma_f32_32x32x16_bf16 v[48:63], v[92:95], v[84:87], v[48:63]\n"
  "ds_read_b64_tr_b16 v[24:25], v118 offset:12352\n"
  "ds_read_b64_tr_b16 v[26:27], v118 offset:13120\n"
  "v_mfma_f32_32x32x16_bf16 v[120:135], v[96:99], v[84:87], v[120:135]\n"
  "ds_read_b64_tr_b16 v[28:29], v118 offset:12416\n"
  "ds_read_b64_tr_b16 v[30:31], v118 offset:13184\n"
  "v_mfma_f32_16x16x32_bf16 v[0:3], v[88:91], v[140:143], v[0:3]\n"
  "v_mfma_f32_16x16x32_bf16 v[4:7], v[92:95], v[140:143], v[4:7]\n"
  "v_mfma_f32_16x16x32_bf16 v[8:11], v[96:99], v[140:143], v[8:11]\n"
  "s_waitcnt lgkmcnt(7)\n"
  "v_mfma_f32_32x32x16_bf16 v[32:47], v[104:107], v[100:103], v[32:47]\n"
  "ds_read_b128 v[84:87], v147 offset:8192\n"
  "ds_read_b64_tr_b16 v[88:89], v119 offset:12288\n"
  "ds_read_b64_tr_b16 v[90:91], v119 offset:13056\n"
  "v_mfma_f32_32x32x16_bf16 v[48:63], v[108:111], v[100:103], v[48:63]\n"
  "ds_read_b64_tr_b16 v[92:93], v119 offset:12352\n"
  "ds_read_b64_tr_b16 v[94:95], v119 offset:13120\n"
  "v_mfma_f32_32x32x16_bf16 v[120:135], v[112:115], v[100:103], v[120:135]\n"
  "ds_read_b64_tr_b16 v[96:97], v119 offset:12416\n"
  "ds_read_b64_tr_b16 v[98:99], v119 offset:13184\n"
  "s_waitcnt lgkmcnt(7)\n"
  "v_mfma_f32_32x32x16_bf16 v[32:47], v[20:23], v[16:19], v[32:47]\n"
  "s_barrier\n"
  "v_mfma_f32_32x32x16_bf16 v[48:63], v[24:27], v[16:19], v[48:63]\n"
  "ds_read_b128 v[100:103], v144 offset:12288\n"
  "ds_read_b64_tr_b16 v[104:105], v116 offset:24576\n"
  "ds_read_b64_tr_b16 v[106:107], v116 offset:25344\n"
  "ds_read_b64_tr_b16 v[108:109], v116 offset:24640\n"
  "v_mfma_f32_32x32x16_bf16 v[120:135], v[28:31], v[16:19], v[120:135]\n"
  "ds_read_b64_tr_b16 v[110:111], v116 offset:25408\n"
  "ds_read_b64_tr_b16 v[112:113], v116 offset:24704\n"
  "ds_read_b64_tr_b16 v[114:115], v116 offset:25472\n"
  "s_waitcnt lgkmcnt(7)\n"
  "v_mfma_f32_32x32x16_bf16 v[32:47], v[88:91], v[84:87], v[32:47]\n"
  "ds_read_b128 v[16:19], v145 offset:12288\n"
  "ds_read_b64_tr_b16 v[20:21], v117 offset:24576\n"
  "ds_read_b64_tr_b16 v[22:23], v117 offset:25344\n"
  "v_mfma_f32_32x32x16_bf16 v[48:63], v[92:95], v[84:87], v[48:63]\n"
  "ds_read_b64_tr_b16 v[24:25], v117 offset:24640\n"
  "ds_read_b64_tr_b16 v[26:27], v117 offset:25408\n"
  "v_mfma_f32_32x32x16_bf16 v[120:135], v[96:99], v[84:87], v[120:135]\n"
  "ds_read_b64_tr_b16 v[28:29], v117 offset:24704\n"
  "ds_read_b64_tr_b16 v[30:31], v117 offset:25472\n"
  "s_waitcnt lgkmcnt(7)\n"
  "v_mfma_f32_32x32x16_bf16 v[32:47], v[104:107], v[100:103], v[32:47]\n"
  "ds_read_b128 v[84:87], v146 offset:12288\n"
  "ds_read_b64_tr_b16 v[88:89], v118 offset:24576\n"
  "ds_read_b64_tr_b16 v[90:91], v118 offset:25344\n"
  "v_mfma_f32_32x32x16_bf16 v[48:63], v[108:111], v[100:103], v[48:63]\n"
  "ds_read_b64_tr_b16 v[92:93], v118 offset:24640\n"
  "ds_read_b64_tr_b16 v[94:95], v118 offset:25408\n"
  "v_mfma_f32_32x32x16_bf16 v[120:135], v[112:115], v[100:103], v[120:135]\n"
  "ds_read_b64_tr_b16 v[96:97], v118 offset:24704\n"
  "ds_read_b64_tr_b16 v[98:99], v118 offset:25472\n"
  "v_mfma_f32_16x16x32_bf16 v[0:3], v[104:107], v[140:143], v[0:3]\n"
  "v_mfma_f32_16x16x32_bf16 v[4:7], v[108:111], v[140:143], v[4:7]\n"
  "v_mfma_f32_16x16x32_bf16 v[8:11], v[112:115], v[140:143], v[8:11]\n"
  "s_waitcnt lgkmcnt(7)\n"
  "v_mfma_f32_32x32x16_bf16 v[32:47], v[20:23], v[16:19], v[32:47]\n"
  "ds_read_b128 v[100:103], v147 offset:12288\n"
  "ds_read_b64_tr_b16 v[104:105], v119 offset:24576\n"
  "ds_read_b64_tr_b16 v[106:107], v119 offset:25344\n"
  "v_mfma_f32_32x32x16_bf16 v[48:63], v[24:27], v[16:19], v[48:63]\n"
  "ds_read_b64_tr_b16 v[108:109], v119 offset:24640\n"
  "ds_read_b64_tr_b16 v[110:111], v119 offset:25408\n"
  "v_mfma_f32_32x32x16_bf16 v[120:135], v[28:31], v[16:19], v[120:135]\n"
  "ds_read_b64_tr_b16 v[112:113], v119 offset:24704\n"
  "ds_read_b64_tr_b16 v[114:115], v119 offset:25472\n"
  "s_waitcnt lgkmcnt(7)\n"
  "v_mfma_f32_32x32x16_bf16 v[32:47], v[88:91], v[84:87], v[32:47]\n"
  "s_barrier\n"
  "v_mfma_f32_32x32x16_bf16 v[48:63], v[92:95], v[84:87], v[48:63]\n"
  "ds_read_b128 v[16:19], v144 offset:16384\n"
  "ds_read_b64_tr_b16 v[20:21], v116 offset:36864\n"
  "ds_read_b64_tr_b16 v[22:23], v116 offset:37632\n"
  "ds_read_b64_tr_b16 v[24:25], v116 offset:36928\n"
  "v_mfma_f32_32x32x16_bf16 v[120:135], v[96:99], v[84:87], v[120:135]\n"
  "ds_read_b64_tr_b16 v[26:27], v116 offset:37696\n"
  "ds_read_b64_tr_b16 v[28:29], v116 offset:36992\n"
  "ds_read_b64_tr_b16 v[30:31], v116 offset:37760\n"
  "s_waitcnt lgkmcnt(7)\n"
  "v_mfma_f32_32x32x16_bf16 v[32:47], v[104:107], v[100:103], v[32:47]\n"
  "ds_read_b128 v[84:87], v145 offset:16384\n"
  "ds_read_b64_tr_b16 v[88:89], v117 offset:36864\n"
  "ds_read_b64_tr_b16 v[90:91], v117 offset:37632\n"
  "v_mfma_f32_32x32x16_bf16 v[48:63], v[108:111], v[100:103], v[48:63]\n"
  "ds_read_b64_tr_b16 v[92:93], v117 offset:36928\n"
  "ds_read_b64_tr_b16 v[94:95], v117 offset:37696\n"
  "v_mfma_f32_32x32x16_bf16 v[120:135], v[112:115], v[100:103], v[120:135]\n"
  "ds_read_b64_tr_b16 v[96:97], v117 offset:36992\n"
  "ds_read_b64_tr_b16 v[98:99], v117 offset:37760\n"
  "s_waitcnt lgkmcnt(7)\n"
  "v_mfma_f32_32x32x16_bf16 v[32:47], v[20:23], v[16:19], v[32:47]\n"
  "ds_read_b128 v[100:103], v146 offset:16384\n"
  "ds_read_b64_tr_b16 v[104:105], v118 offset:36864\n"
  "ds_read_b64_tr_b16 v[106:107], v118 offset:37632\n"
  "v_mfma_f32_32x32x16_bf16 v[48:63], v[24:27], v[16:19], v[48:63]\n"
  "ds_read_b64_tr_b16 v[108:109], v118 offset:36928\n"
  "ds_read_b64_tr_b16 v[110:111], v118 offset:37696\n"
  "v_mfma_f32_32x32x16_bf16 v[120:135], v[28:31], v[16:19], v[120:135]\n"
  "ds_read_b64_tr_b16 v[112:113], v118 offset:36992\n"
  "ds_read_b64_tr_b16 v[114:115], v118 offset:37760\n"
  "v_mfma_f32_16x16x32_bf16 v[0:3], v[20:23], v[140:143], v[0:3]\n"
  "v_mfma_f32_16x16x32_bf16 v[4:7], v[24:27], v[140:143], v[4:7]\n"
  "v_mfma_f32_16x16x32_bf16 v[8:11], v[28:31], v[140:143], v[8:11]\n"
  "s_waitcnt lgkmcnt(7)\n"
  "v_mfma_f32_32x32x16_bf16 v[32:47], v[88:91], v[84:87], v[32:47]\n"
  "ds_read_b128 v[16:19], v147 offset:16384\n"
  "ds_read_b64_tr_b16 v[20:21], v119 offset:36864\n"
  "ds_read_b64_tr_b16 v[22:23], v119 offset:37632\n"
  "v_mfma_f32_32x32x16_bf16 v[48:63], v[92:95], v[84:87], v[48:63]\n"
  "ds_read_b64_tr_b16 v[24:25], v119 offset:36928\n"
  "ds_read_b64_tr_b16 v[26:27], v119 offset:37696\n"
  "v_mfma_f32_32x32x16_bf16 v[120:135], v[96:99], v[84:87], v[120:135]\n"
  "ds_read_b64_tr_b16 v[28:29], v119 offset:36992\n"
  "ds_read_b64_tr_b16 v[30:31], v119 offset:37760\n"
  "s_waitcnt lgkmcnt(7)\n"
  "v_mfma_f32_32x32x16_bf16 v[32:47], v[104:107], v[100:103], v[32:47]\n"
  "s_barrier\n"
  "v_mfma_f32_32x32x16_bf16 v[48:63], v[108:111], v[100:103], v[48:63]\n"
  "ds_read_b128 v[84:87], v144 offset:0\n"
  "ds_read_b64_tr_b16 v[88:89], v116 offset:49152\n"
  "ds_read_b64_tr_b16 v[90:91], v116 offset:49920\n"
  "ds_read_b64_tr_b16 v[92:93], v116 offset:49216\n"
  "v_mfma_f32_32x32x16_bf16 v[120:135], v[112:115], v[100:103], v[120:135]\n"
  "ds_read_b64_tr_b16 v[94:95], v116 offset:49984\n"
  "ds_read_b64_tr_b16 v[96:97], v116 offset:49280\n"
  "ds_read_b64_tr_b16 v[98:99], v116 offset:50048\n"
  "s_waitcnt lgkmcnt(7)\n"
  "v_mfma_f32_32x32x16_bf16 v[32:47], v[20:23], v[16:19], v[32:47]\n"
  "ds_read_b128 v[100:103], v145 offset:0\n"
  "ds_read_b64_tr_b16 v[104:105], v117 offset:49152\n"
  "ds_read_b64_tr_b16 v[106:107], v117 offset:49920\n"
  "v_mfma_f32_32x32x16_bf16 v[48:63], v[24:27], v[16:19], v[48:63]\n"
  "ds_read_b64_tr_b16 v[108:109], v117 offset:49216\n"
  "ds_read_b64_tr_b16 v[110:111], v117 offset:49984\n"
  "v_mfma_f32_32x32x16_bf16 v[120:135], v[28:31], v[16:19], v[120:135]\n"
  "ds_read_b64_tr_b16 v[112:113], v117 offset:49280\n"
  "ds_read_b64_tr_b16 v[114:115], v117 offset:50048\n"
  "s_waitcnt lgkmcnt(7)\n"
  "v_mfma_f32_32x32x16_bf16 v[32:47], v[88:91], v[84:87], v[32:47]\n"
  "ds_read_b128 v[16:19], v146 offset:0\n"
  "ds_read_b64_tr_b16 v[20:21], v118 offset:49152\n"
  "ds_read_b64_tr_b16 v[22:23], v118 offset:49920\n"
  "v_mfma_f32_32x32x16_bf16 v[48:63], v[92:95], v[84:87], v[48:63]\n"
  "ds_read_b64_tr_b16 v[24:25], v118 offset:49216\n"
  "ds_read_b64_tr_b16 v[26:27], v118 offset:49984\n"
  "v_mfma_f32_32x32x16_bf16 v[120:135], v[96:99], v[84:87], v[120:135]\n"
  "ds_read_b64_tr_b16 v[28:29], v118 offset:49280\n"
  "ds_read_b64_tr_b16 v[30:31], v118 offset:50048\n"
  "v_mfma_f32_16x16x32_bf16 v[0:3], v[88:91], v[140:143], v[0:3]\n"
  "v_mfma_f32_16x16x32_bf16 v[4:7], v[92:95], v[140:143], v[4:7]\n"
  "v_mfma_f32_16x16x32_bf16 v[8:11], v[96:99], v[140:143], v[8:11]\n"
  "s_waitcnt lgkmcnt(7)\n"
  "v_mfma_f32_32x32x16_bf16 v[32:47], v[104:107], v[100:103], v[32:47]\n"
  "ds_read_b128 v[84:87], v147 offset:0\n"
  "ds_read_b64_tr_b16 v[88:89], v119 offset:49152\n"
  "ds_read_b64_tr_b16 v[90:91], v119 offset:49920\n"
  "v_mfma_f32_32x32x16_bf16 v[48:63], v[108:111], v[100:103], v[48:63]\n"
  "ds_read_b64_tr_b16 v[92:93], v119 offset:49216\n"
  "ds_read_b64_tr_b16 v[94:95], v119 offset:49984\n"
  "v_mfma_f32_32x32x16_bf16 v[120:135], v[112:115], v[100:103], v[120:135]\n"
  "ds_read_b64_tr_b16 v[96:97], v119 offset:49280\n"
  "ds_read_b64_tr_b16 v[98:99], v119 offset:50048\n"
  "s_waitcnt lgkmcnt(7)\n"
  "v_mfma_f32_32x32x16_bf16 v[32:47], v[20:23], v[16:19], v[32:47]\n"
  "s_barrier\n"
  "v_mfma_f32_32x32x16_bf16 v[48:63], v[24:27], v[16:19], v[48:63]\n"
  "ds_read_b128 v[100:103], v144 offset:4096\n"
  "ds_read_b64_tr_b16 v[104:105], v116 offset:61440\n"
  "ds_read_b64_tr_b16 v[106:107], v116 offset:62208\n"
  "ds_read_b64_tr_b16 v[108:109], v116 offset:61504\n"
  "v_mfma_f32_32x32x16_bf16 v[120:135], v[28:31], v[16:19], v[120:135]\n"
  "ds_read_b64_tr_b16 v[110:111], v116 offset:62272\n"
  "ds_read_b64_tr_b16 v[112:113], v116 offset:61568\n"
  "ds_read_b64_tr_b16 v[114:115], v116 offset:62336\n"
  "s_waitcnt lgkmcnt(7)\n"
  "v_mfma_f32_32x32x16_bf16 v[32:47], v[88:91], v[84:87], v[32:47]\n"
  "ds_read_b128 v[16:19], v145 offset:4096\n"
  "ds_read_b64_tr_b16 v[20:21], v117 offset:61440\n"
  "ds_read_b64_tr_b16 v[22:23], v117 offset:62208\n"
  "v_mfma_f32_32x32x16_bf16 v[48:63], v[92:95], v[84:87], v[48:63]\n"
  "ds_read_b64_tr_b16 v[24:25], v117 offset:61504\n"
  "ds_read_b64_tr_b16 v[26:27], v117 offset:62272\n"
  "v_mfma_f32_32x32x16_bf16 v[120:135], v[96:99], v[84:87], v[120:135]\n"
  "ds_read_b64_tr_b16 v[28:29], v117 offset:61568\n"
  "ds_read_b64_tr_b16 v[30:31], v117 offset:62336\n"
  "s_waitcnt lgkmcnt(7)\n"
  "v_mfma_f32_32x32x16_bf16 v[32:47], v[104:107], v[100:103], v[32:47]\n"
  "ds_read_b128 v[84:87], v146 offset:4096\n"
  "ds_read_b64_tr_b16 v[88:89], v118 offset:61440\n"
  "ds_read_b64_tr_b16 v[90:91], v118 offset:62208\n"
  "v_mfma_f32_32x32x16_bf16 v[48:63], v[108:111], v[100:103], v[48:63]\n"
  "ds_read_b64_tr_b16 v[92:93], v118 offset:61504\n"
  "ds_read_b64_tr_b16 v[94:95], v118 offset:62272\n"
  "v_mfma_f32_32x32x16_bf16 v[120:135], v[112:115], v[100:103], v[120:135]\n"
  "ds_read_b64_tr_b16 v[96:97], v118 offset:61568\n"
  "ds_read_b64_tr_b16 v[98:99], v118 offset:62336\n"
  "v_mfma_f32_16x16x32_bf16 v[0:3], v[104:107], v[140:143], v[0:3]\n"
  "v_mfma_f32_16x16x32_bf16 v[4:7], v[108:111], v[140:143], v[4:7]\n"
  "v_mfma_f32_16x16x32_bf16 v[8:11], v[112:115], v[140:143], v[8:11]\n"
  "s_waitcnt lgkmcnt(7)\n"
  "v_mfma_f32_32x32x16_bf16 v[32:47], v[20:23], v[16:19], v[32:47]\n"
  "ds_read_b128 v[100:103], v147 offset:4096\n"
  "ds_read_b64_tr_b16 v[104:105], v119 offset:61440\n"
  "ds_read_b64_tr_b16 v[106:107], v119 offset:62208\n"
  "v_mfma_f32_32x32x16_bf16 v[48:63], v[24:27], v[16:19], v[48:63]\n"
  "ds_read_b64_tr_b16 v[108:109], v119 offset:61504\n"
  "ds_read_b64_tr_b16 v[110:111], v119 offset:62272\n"
  "v_mfma_f32_32x32x16_bf16 v[120:135], v[28:31], v[16:19], v[120:135]\n"
  "ds_read_b64_tr_b16 v[112:113], v119 offset:61568\n"
  "ds_read_b64_tr_b16 v[114:115], v119 offset:62336\n"
  "s_waitcnt lgkmcnt(7)\n"
  "v_mfma_f32_32x32x16_bf16 v[32:47], v[88:91], v[84:87], v[32:47]\n"
  "s_barrier\n"
  "v_mfma_f32_32x32x16_bf16 v[48:63], v[92:95], v[84:87], v[48:63]\n"
  "ds_read_b128 v[16:19], v144 offset:8192\n"
  "ds_read_b64_tr_b16 v[20:21], v116 offset:0\n"
  "ds_read_b64_tr_b16 v[22:23], v116 offset:768\n"
  "ds_read_b64_tr_b16 v[24:25], v116 offset:64\n"
  "v_mfma_f32_32x32x16_bf16 v[120:135], v[96:99], v[84:87], v[120:135]\n"
  "ds_read_b64_tr_b16 v[26:27], v116 offset:832\n"
  "ds_read_b64_tr_b16 v[28:29], v116 offset:128\n"
  "ds_read_b64_tr_b16 v[30:31], v116 offset:896\n"
  "s_waitcnt lgkmcnt(7)\n"
  "v_mfma_f32_32x32x16_bf16 v[32:47], v[104:107], v[100:103], v[32:47]\n"
  "ds_read_b128 v[84:87], v145 offset:8192\n"
  "ds_read_b64_tr_b16 v[88:89], v117 offset:0\n"
  "ds_read_b64_tr_b16 v[90:91], v117 offset:768\n"
  "v_mfma_f32_32x32x16_bf16 v[48:63], v[108:111], v[100:103], v[48:63]\n"
  "ds_read_b64_tr_b16 v[92:93], v117 offset:64\n"
  "ds_read_b64_tr_b16 v[94:95], v117 offset:832\n"
  "v_mfma_f32_32x32x16_bf16 v[120:135], v[112:115], v[100:103], v[120:135]\n"
  "ds_read_b64_tr_b16 v[96:97], v117 offset:128\n"
  "ds_read_b64_tr_b16 v[98:99], v117 offset:896\n"
  "s_waitcnt lgkmcnt(7)\n"
  "v_mfma_f32_32x32x16_bf16 v[32:47], v[20:23], v[16:19], v[32:47]\n"
  "ds_read_b128 v[100:103], v146 offset:8192\n"
  "ds_read_b64_tr_b16 v[104:105], v118 offset:0\n"
  "ds_read_b64_tr_b16 v[106:107], v118 offset:768\n"
  "v_mfma_f32_32x32x16_bf16 v[48:63], v[24:27], v[16:19], v[48:63]\n"
  "ds_read_b64_tr_b16 v[108:109], v118 offset:64\n"
  "ds_read_b64_tr_b16 v[110:111], v118 offset:832\n"
  "v_mfma_f32_32x32x16_bf16 v[120:135], v[28:31], v[16:19], v[120:135]\n"
  "ds_read_b64_tr_b16 v[112:113], v118 offset:128\n"
  "ds_read_b64_tr_b16 v[114:115], v118 offset:896\n"
  "v_mfma_f32_16x16x32_bf16 v[0:3], v[20:23], v[140:143], v[0:3]\n"
  "v_mfma_f32_16x16x32_bf16 v[4:7], v[24:27], v[140:143], v[4:7]\n"
  "v_mfma_f32_16x16x32_bf16 v[8:11], v[28:31], v[140:143], v[8:11]\n"
  "s_waitcnt lgkmcnt(7)\n"
  "v_mfma_f32_32x32x16_bf16 v[32:47], v[88:91], v[84:87], v[32:47]\n"
  "ds_read_b128 v[16:19], v147 offset:8192\n"
  "ds_read_b64_tr_b16 v[20:21], v119 offset:0\n"
  "ds_read_b64_tr_b16 v[22:23], v119 offset:768\n"
  "v_mfma_f32_32x32x16_bf16 v[48:63], v[92:95], v[84:87], v[48:63]\n"
  "ds_read_b64_tr_b16 v[24:25], v119 offset:64\n"
  "ds_read_b64_tr_b16 v[26:27], v119 offset:832\n"
  "v_mfma_f32_32x32x16_bf16 v[120:135], v[96:99], v[84:87], v[120:135]\n"
  "ds_read_b64_tr_b16 v[28:29], v119 offset:128\n"
  "ds_read_b64_tr_b16 v[30:31], v119 offset:896\n"
  "s_waitcnt lgkmcnt(7)\n"
  "v_mfma_f32_32x32x16_bf16 v[32:47], v[104:107], v[100:103], v[32:47]\n"
  "s_barrier\n"
  "v_mfma_f32_32x32x16_bf16 v[48:63], v[108:111], v[100:103], v[48:63]\n"
  "ds_read_b128 v[84:87], v144 offset:12288\n"
  "ds_read_b64_tr_b16 v[88:89], v116 offset:12288\n"
  "ds_read_b64_tr_b16 v[90:91], v116 offset:13056\n"
  "ds_read_b64_tr_b16 v[92:93], v116 offset:12352\n"
  "v_mfma_f32_32x32x16_bf16 v[120:135], v[112:115], v[100:103], v[120:135]\n"
  "ds_read_b64_tr_b16 v[94:95], v116 offset:13120\n"
  "ds_read_b64_tr_b16 v[96:97], v116 offset:12416\n"
  "ds_read_b64_tr_b16 v[98:99], v116 offset:13184\n"
  "s_waitcnt lgkmcnt(7)\n"
  "v_mfma_f32_32x32x16_bf16 v[32:47], v[20:23], v[16:19], v[32:47]\n"
  "ds_read_b128 v[100:103], v145 offset:12288\n"
  "ds_read_b64_tr_b16 v[104:105], v117 offset:12288\n"
  "ds_read_b64_tr_b16 v[106:107], v117 offset:13056\n"
  "v_mfma_f32_32x32x16_bf16 v[48:63], v[24:27], v[16:19], v[48:63]\n"
  "ds_read_b64_tr_b16 v[108:109], v117 offset:12352\n"
  "ds_read_b64_tr_b16 v[110:111], v117 offset:13120\n"
  "v_mfma_f32_32x32x16_bf16 v[120:135], v[28:31], v[16:19], v[120:135]\n"
  "ds_read_b64_tr_b16 v[112:113], v117 offset:12416\n"
  "ds_read_b64_tr_b16 v[114:115], v117 offset:13184\n"
  "s_waitcnt lgkmcnt(7)\n"
  "v_mfma_f32_32x32x16_bf16 v[32:47], v[88:91], v[84:87], v[32:47]\n"
  "ds_read_b128 v[16:19], v146 offset:12288\n"
  "ds_read_b64_tr_b16 v[20:21], v118 offset:12288\n"
  "ds_read_b64_tr_b16 v[22:23], v118 offset:13056\n"
  "v_mfma_f32_32x32x16_bf16 v[48:63], v[92:95], v[84:87], v[48:63]\n"
  "ds_read_b64_tr_b16 v[24:25], v118 offset:12352\n"
  "ds_read_b64_tr_b16 v[26:27], v118 offset:13120\n"
  "v_mfma_f32_32x32x16_bf16 v[120:135], v[96:99], v[84:87], v[120:135]\n"
  "ds_read_b64_tr_b16 v[28:29], v118 offset:12416\n"
  "ds_read_b64_tr_b16 v[30:31], v118 offset:13184\n"
  "v_mfma_f32_16x16x32_bf16 v[0:3], v[88:91], v[140:143], v[0:3]\n"
  "v_mfma_f32_16x16x32_bf16 v[4:7], v[92:95], v[140:143], v[4:7]\n"
  "v_mfma_f32_16x16x32_bf16 v[8:11], v[96:99], v[140:143], v[8:11]\n"
  "s_waitcnt lgkmcnt(7)\n"
  "v_mfma_f32_32x32x16_bf16 v[32:47], v[104:107], v[100:103], v[32:47]\n"
  "ds_read_b128 v[84:87], v147 offset:12288\n"
  "ds_read_b64_tr_b16 v[88:89], v119 offset:12288\n"
  "ds_read_b64_tr_b16 v[90:91], v119 offset:13056\n"
  "v_mfma_f32_32x32x16_bf16 v[48:63], v[108:111], v[100:103], v[48:63]\n"
  "ds_read_b64_tr_b16 v[92:93], v119 offset:12352\n"
  "ds_read_b64_tr_b16 v[94:95], v119 offset:13120\n"
  "v_mfma_f32_32x32x16_bf16 v[120:135], v[112:115], v[100:103], v[120:135]\n"
  "ds_read_b64_tr_b16 v[96:97], v119 offset:12416\n"
  "ds_read_b64_tr_b16 v[98:99], v119 offset:13184\n"
  "s_waitcnt lgkmcnt(7)\n"
  "v_mfma_f32_32x32x16_bf16 v[32:47], v[20:23], v[16:19], v[32:47]\n"
  "s_barrier\n"
  "v_mfma_f32_32x32x16_bf16 v[48:63], v[24:27], v[16:19], v[48:63]\n"
  "ds_read_b128 v[100:103], v144 offset:16384\n"
  "ds_read_b64_tr_b16 v[104:105], v116 offset:24576\n"
  "ds_read_b64_tr_b16 v[106:107], v116 offset:25344\n"
  "ds_read_b64_tr_b16 v[108:109], v116 offset:24640\n"
  "v_mfma_f32_32x32x16_bf16 v[120:135], v[28:31], v[16:19], v[120:135]\n"
  "ds_read_b64_tr_b16 v[110:111], v116 offset:25408\n"
  "ds_read_b64_tr_b16 v[112:113], v116 offset:24704\n"
  "ds_read_b64_tr_b16 v[114:115], v116 offset:25472\n"
  "s_waitcnt lgkmcnt(7)\n"
  "v_mfma_f32_32x32x16_bf16 v[32:47], v[88:91], v[84:87], v[32:47]\n"
  "ds_read_b128 v[16:19], v145 offset:16384\n"
  "ds_read_b64_tr_b16 v[20:21], v117 offset:24576\n"
  "ds_read_b64_tr_b16 v[22:23], v117 offset:25344\n"
  "v_mfma_f32_32x32x16_bf16 v[48:63], v[92:95], v[84:87], v[48:63]\n"
  "ds_read_b64_tr_b16 v[24:25], v117 offset:24640\n"
  "ds_read_b64_tr_b16 v[26:27], v117 offset:25408\n"
  "v_mfma_f32_32x32x16_bf16 v[120:135], v[96:99], v[84:87], v[120:135]\n"
  "ds_read_b64_tr_b16 v[28:29], v117 offset:24704\n"
  "ds_read_b64_tr_b16 v[30:31], v117 offset:25472\n"
  "s_waitcnt lgkmcnt(7)\n"
  "v_mfma_f32_16x16x32_bf16 v[0:3], v[104:107], v[140:143], v[0:3]\n"
  "v_mfma_f32_16x16x32_bf16 v[4:7], v[108:111], v[140:143], v[4:7]\n"
  "v_mfma_f32_16x16x32_bf16 v[8:11], v[112:115], v[140:143], v[8:11]\n"
  "v_mfma_f32_32x32x16_bf16 v[32:47], v[104:107], v[100:103], v[32:47]\n"
  "ds_read_b128 v[84:87], v146 offset:16384\n"
  "ds_read_b64_tr_b16 v[88:89], v118 offset:24576\n"
  "ds_read_b64_tr_b16 v[90:91], v118 offset:25344\n"
  "v_mfma_f32_32x32x16_bf16 v[48:63], v[108:111], v[100:103], v[48:63]\n"
  "ds_read_b64_tr_b16 v[92:93], v118 offset:24640\n"
  "ds_read_b64_tr_b16 v[94:95], v118 offset:25408\n"
  "v_mfma_f32_32x32x16_bf16 v[120:135], v[112:115], v[100:103], v[120:135]\n"
  "ds_read_b64_tr_b16 v[96:97], v118 offset:24704\n"
  "ds_read_b64_tr_b16 v[98:99], v118 offset:25472\n"
  "s_waitcnt lgkmcnt(7)\n"
  "v_mfma_f32_32x32x16_bf16 v[32:47], v[20:23], v[16:19], v[32:47]\n"
  "ds_read_b128 v[100:103], v147 offset:16384\n"
  "ds_read_b64_tr_b16 v[104:105], v119 offset:24576\n"
  "ds_read_b64_tr_b16 v[106:107], v119 offset:25344\n"
  "v_mfma_f32_32x32x16_bf16 v[48:63], v[24:27], v[16:19], v[48:63]\n"
  "ds_read_b64_tr_b16 v[108:109], v119 offset:24640\n"
  "ds_read_b64_tr_b16 v[110:111], v119 offset:25408\n"
  "s_mov_b32 exec_lo, 0x30003\n"
  "s_mov_b32 exec_hi, 0x30003\n"
  "ds_write_b128 v148, v[0:3] offset:0\n"
  "ds_write_b128 v148, v[4:7] offset:512\n"
  "ds_write_b128 v148, v[8:11] offset:1024\n"
  "s_mov_b64 exec, -1\n"
  "v_mfma_f32_32x32x16_bf16 v[120:135], v[28:31], v[16:19], v[120:135]\n"
  "s_waitcnt lgkmcnt(12)\n"
  "ds_read_b64_tr_b16 v[112:113], v119 offset:24704\n"
  "ds_read_b64_tr_b16 v[114:115], v119 offset:25472\n"
  "s_waitcnt lgkmcnt(10)\n"
  "v_mfma_f32_32x32x16_bf16 v[32:47], v[88:91], v[84:87], v[32:47]\n"
  "s_waitcnt lgkmcnt(0)\n"
  "s_barrier\n"
  "ds_read_b32 v0, v149 offset:0\n"
  "ds_read_b32 v1, v149 offset:128\n"
  "ds_read_b32 v4, v149 offset:512\n"
  "ds_read_b32 v5, v149 offset:640\n"
  "ds_read_b32 v8, v149 offset:1024\n"
  "ds_read_b32 v9, v149 offset:1152\n"
  "v_mfma_f32_32x32x16_bf16 v[48:63], v[92:95], v[84:87], v[48:63]\n"
  "v_mfma_f32_32x32x16_bf16 v[120:135], v[96:99], v[84:87], v[120:135]\n"
  "s_waitcnt lgkmcnt(0)\n"
  "v_add_f32 v2, v0, v1\n"
  "v_cvt_pk_bf16_f32 v0, v2, v2\n"
  "v_and_b32 v3, 0xffff0000, v0\n"
  "v_sub_f32 v2, v2, v3\n"
  "v_cvt_pk_bf16_f32 v1, v2, v2\n"
  "v_mfma_f32_32x32x16_bf16 v[32:47], v[104:107], v[100:103], v[32:47]\n"
  "v_add_f32 v6, v4, v5\n"
  "v_cvt_pk_bf16_f32 v4, v6, v6\n"
  "v_and_b32 v7, 0xffff0000, v4\n"
  "v_sub_f32 v6, v6, v7\n"
  "v_cvt_pk_bf16_f32 v5, v6, v6\n"
  "v_mfma_f32_32x32x16_bf16 v[48:63], v[108:111], v[100:103], v[48:63]\n"
  "v_add_f32 v10, v8, v9\n"
  "v_cvt_pk_bf16_f32 v8, v10, v10\n"
  "v_and_b32 v11, 0xffff0000, v8\n"
  "v_sub_f32 v10, v10, v11\n"
  "v_cvt_pk_bf16_f32 v9, v10, v10\n"
  "v_mfma_f32_32x32x16_bf16 v[120:135], v[112:115], v[100:103], v[120:135]\n"
  "s_nop 1\n"
  "v_mfma_f32_32x32x8_bf16 v[32:47], v[0:1], v[136:137], v[32:47]\n"
  "v_mfma_f32_32x32x8_bf16 v[48:63], v[4:5], v[136:137], v[48:63]\n"
  "v_mfma_f32_32x32x8_bf16 v[120:135], v[8:9], v[136:137], v[120:135]\n"
  "v_mov_b32 v138, v14\n"
  "v_mov_b64 v[0:1], 0\n"
  "v_mov_b64 v[2:3], 0\n"
  "v_mov_b64 v[4:5], 0\n"
  "v_mov_b64 v[6:7], 0\n"
  "v_mov_b64 v[8:9], 0\n"
  "v_mov_b64 v[10:11], 0\n"
  "v_mov_b64 v[12:13], 0\n"
  "v_mov_b64 v[14:15], 0\n"
  "v_mov_b64 v[16:17], 0\n"
  "v_mov_b64 v[18:19], 0\n"
  "v_mov_b64 v[20:21], 0\n"
  "v_mov_b64 v[22:23], 0\n"
  "v_mov_b64 v[24:25], 0\n"
  "v_mov_b64 v[26:27], 0\n"
  "v_mov_b64 v[28:29], 0\n"
  "v_mov_b64 v[30:31], 0\n"
  "v_pk_mul_f32 v[32:33], v[32:33], v[138:139] op_sel_hi:[1,0]\n"
  "v_pk_mul_f32 v[34:35], v[34:35], v[138:139] op_sel_hi:[1,0]\n"
  "v_pk_mul_f32 v[36:37], v[36:37], v[138:139] op_sel_hi:[1,0]\n"
  "v_pk_mul_f32 v[38:39], v[38:39], v[138:139] op_sel_hi:[1,0]\n"
  "v_pk_mul_f32 v[40:41], v[40:41], v[138:139] op_sel_hi:[1,0]\n"
  "v_pk_mul_f32 v[42:43], v[42:43], v[138:139] op_sel_hi:[1,0]\n"
  "v_pk_mul_f32 v[44:45], v[44:45], v[138:139] op_sel_hi:[1,0]\n"
  "v_pk_mul_f32 v[46:47], v[46:47], v[138:139] op_sel_hi:[1,0]\n"
  "v_cvt_pk_bf16_f32 v104, v32, v33\n"
  "v_cvt_pk_bf16_f32 v105, v34, v35\n"
  "v_cvt_pk_bf16_f32 v106, v36, v37\n"
  "v_cvt_pk_bf16_f32 v107, v38, v39\n"
  "v_cvt_pk_bf16_f32 v108, v40, v41\n"
  "v_cvt_pk_bf16_f32 v109, v42, v43\n"
  "v_cvt_pk_bf16_f32 v110, v44, v45\n"
  "v_cvt_pk_bf16_f32 v111, v46, v47\n"
  "v_pk_max_i16 v104, v104, 0\n"
  "v_pk_max_i16 v105, v105, 0\n"
  "v_pk_max_i16 v106, v106, 0\n"
  "v_pk_max_i16 v107, v107, 0\n"
  "v_pk_max_i16 v108, v108, 0\n"
  "v_pk_max_i16 v109, v109, 0\n"
  "v_pk_max_i16 v110, v110, 0\n"
  "v_pk_max_i16 v111, v111, 0\n"
  "v_pk_mul_f32 v[48:49], v[48:49], v[138:139] op_sel_hi:[1,0]\n"
  "v_pk_mul_f32 v[50:51], v[50:51], v[138:139] op_sel_hi:[1,0]\n"
  "v_pk_mul_f32 v[52:53], v[52:53], v[138:139] op_sel_hi:[1,0]\n"
  "v_pk_mul_f32 v[54:55], v[54:55], v[138:139] op_sel_hi:[1,0]\n"
  "v_pk_mul_f32 v[56:57], v[56:57], v[138:139] op_sel_hi:[1,0]\n"
  "v_pk_mul_f32 v[58:59], v[58:59], v[138:139] op_sel_hi:[1,0]\n"
  "v_pk_mul_f32 v[60:61], v[60:61], v[138:139] op_sel_hi:[1,0]\n"
  "v_pk_mul_f32 v[62:63], v[62:63], v[138:139] op_sel_hi:[1,0]\n"
  "v_cvt_pk_bf16_f32 v112, v48, v49\n"
  "v_cvt_pk_bf16_f32 v113, v50, v51\n"
  "v_cvt_pk_bf16_f32 v114, v52, v53\n"
  "v_cvt_pk_bf16_f32 v115, v54, v55\n"
  "v_cvt_pk_bf16_f32 v116, v56, v57\n"
  "v_cvt_pk_bf16_f32 v117, v58, v59\n"
  "v_cvt_pk_bf16_f32 v118, v60, v61\n"
  "v_cvt_pk_bf16_f32 v119, v62, v63\n"
  "v_pk_max_i16 v112, v112, 0\n"
  "v_pk_max_i16 v113, v113, 0\n"
  "v_pk_max_i16 v114, v114, 0\n"
  "v_pk_max_i16 v115, v115, 0\n"
  "v_pk_max_i16 v116, v116, 0\n"
  "v_pk_max_i16 v117, v117, 0\n"
  "v_pk_max_i16 v118, v118, 0\n"
  "v_pk_max_i16 v119, v119, 0\n"
  "v_pk_mul_f32 v[120:121], v[120:121], v[138:139] op_sel_hi:[1,0]\n"
  "v_pk_mul_f32 v[122:123], v[122:123], v[138:139] op_sel_hi:[1,0]\n"
  "v_pk_mul_f32 v[124:125], v[124:125], v[138:139] op_sel_hi:[1,0]\n"
  "v_pk_mul_f32 v[126:127], v[126:127], v[138:139] op_sel_hi:[1,0]\n"
  "v_pk_mul_f32 v[128:129], v[128:129], v[138:139] op_sel_hi:[1,0]\n"
  "v_pk_mul_f32 v[130:131], v[130:131], v[138:139] op_sel_hi:[1,0]\n"
  "v_pk_mul_f32 v[132:133], v[132:133], v[138:139] op_sel_hi:[1,0]\n"
  "v_pk_mul_f32 v[134:135], v[134:135], v[138:139] op_sel_hi:[1,0]\n"
  "v_cvt_pk_bf16_f32 v74, v120, v121\n"
  "v_cvt_pk_bf16_f32 v75, v122, v123\n"
  "v_cvt_pk_bf16_f32 v76, v124, v125\n"
  "v_cvt_pk_bf16_f32 v77, v126, v127\n"
  "v_cvt_pk_bf16_f32 v78, v128, v129\n"
  "v_cvt_pk_bf16_f32 v79, v130, v131\n"
  "v_cvt_pk_bf16_f32 v80, v132, v133\n"
  "v_cvt_pk_bf16_f32 v81, v134, v135\n"
  "v_pk_max_i16 v74, v74, 0\n"
  "v_pk_max_i16 v75, v75, 0\n"
  "v_pk_max_i16 v76, v76, 0\n"
  "v_pk_max_i16 v77, v77, 0\n"
  "v_pk_max_i16 v78, v78, 0\n"
  "v_pk_max_i16 v79, v79, 0\n"
  "v_pk_max_i16 v80, v80, 0\n"
  "v_pk_max_i16 v81, v81, 0\n"
  "9:\n"
  "s_barrier\n"
  "ds_read_b128 v[136:139], v156 offset:4096\n"
  "ds_read_b128 v[150:153], v72\n"
  "ds_read_b64 v[142:143], v164\n"
  "ds_read_b64 v[154:155], v73\n"
  "ds_read_b128 v[144:147], v82\n"
  "ds_read_b64 v[148:149], v83\n"
  "ds_read_b128 v[100:103], v165\n"
  "ds_read_b64 v[140:141], v166\n"
  "s_waitcnt lgkmcnt(4)\n"
  "v_mfma_f32_32x32x16_bf16 v[120:135], v[136:139], v[150:153], 0\n"
  "v_mfma_f32_32x32x8_bf16 v[120:135], v[142:143], v[154:155], v[120:135]\n"
  "s_waitcnt lgkmcnt(2)\n"
  "v_mfma_f32_32x32x16_bf16 v[32:47], v[136:139], v[144:147], 0\n"
  "v_mfma_f32_32x32x8_bf16 v[32:47], v[142:143], v[148:149], v[32:47]\n"
  "s_nop 8\n"
  "v_max3_f32 v120, v120, v121, v122\n"
  "v_max3_f32 v123, v123, v124, v125\n"
  "v_max3_f32 v126, v126, v127, v128\n"
  "v_max3_f32 v129, v129, v130, v131\n"
  "v_max3_f32 v132, v132, v133, v134\n"
  "v_max3_f32 v120, v120, v123, v135\n"
  "v_max3_f32 v126, v126, v129, v132\n"
  "v_max_f32 v120, v120, v126\n"
  "v_mov_b32 v121, v120\n"
  "s_nop 1\n"
  "v_permlane32_swap_b32 v120, v121\n"
  "v_max_f32 v120, v120, v121\n"
  "v_cvt_pk_bf16_f32 v120, -v120, 0\n"
  "v_cndmask_b32 v154, v154, v120, s[36:37]\n"
  "v_max3_f32 v32, v32, v33, v34\n"
  "v_max3_f32 v35, v35, v36, v37\n"
  "v_max3_f32 v38, v38, v39, v40\n"
  "v_max3_f32 v41, v41, v42, v43\n"
  "v_max3_f32 v44, v44, v45, v46\n"
  "v_max3_f32 v32, v32, v35, v47\n"
  "v_max3_f32 v38, v38, v41, v44\n"
  "v_max_f32 v32, v32, v38\n"
  "v_mov_b32 v33, v32\n"
  "s_nop 1\n"
  "v_permlane32_swap_b32 v32, v33\n"
  "v_max_f32 v32, v32, v33\n"
  "v_cvt_pk_bf16_f32 v32, -v32, 0\n"
  "v_cndmask_b32 v148, v148, v32, s[36:37]\n"
  "s_waitcnt lgkmcnt(0)\n"
  "v_mfma_f32_32x32x16_bf16 v[32:47], v[100:103], v[150:153], 0\n"
  "v_mfma_f32_32x32x8_bf16 v[32:47], v[140:141], v[154:155], v[32:47]\n"
  "s_nop 9\n"
  "v_mfma_f32_32x32x16_bf16 v[48:63], v[100:103], v[144:147], 0\n"
  "ds_read_b128 v[136:139], v158 offset:0\n"
  "s_setprio 3\n"
  "v_exp_f32 v32, v32\n"
  "v_exp_f32 v33, v33\n"
  "v_exp_f32 v34, v34\n"
  "v_mfma_f32_32x32x8_bf16 v[48:63], v[140:141], v[148:149], v[48:63]\n"
  "ds_read_b64 v[142:143], v159 offset:0\n"
  "v_exp_f32 v35, v35\n"
  "v_exp_f32 v36, v36\n"
  "v_exp_f32 v37, v37\n"
  "v_exp_f32 v38, v38\n"
  "ds_read_b64_tr_b16 v[84:85], v64 offset:0\n"
  "ds_read_b64_tr_b16 v[86:87], v65 offset:0\n"
  "v_exp_f32 v39, v39\n"
  "v_exp_f32 v40, v40\n"
  "v_exp_f32 v41, v41\n"
  "ds_read_b64_tr_b16 v[88:89], v66 offset:0\n"
  "ds_read_b64_tr_b16 v[90:91], v67 offset:0\n"
  "v_exp_f32 v42, v42\n"
  "v_exp_f32 v43, v43\n"
  "v_exp_f32 v44, v44\n"
  "buffer_store_dwordx4 v[104:107], v156, s[16:19], s65 offen offset:0 sc1\n"
  "v_exp_f32 v45, v45\n"
  "v_exp_f32 v46, v46\n"
  "v_exp_f32 v47, v47\n"
  "s_waitcnt lgkmcnt(4)\n"
  "v_mfma_f32_32x32x16_bf16 v[120:135], v[136:139], v[150:153], 0\n"
  "s_setprio 2\n"
  "v_exp_f32 v48, v48\n"
  "v_exp_f32 v49, v49\n"
  "v_exp_f32 v50, v50\n"
  "v_mfma_f32_32x32x8_bf16 v[120:135], v[142:143], v[154:155], v[120:135]\n"
  "v_exp_f32 v51, v51\n"
  "v_exp_f32 v52, v52\n"
  "buffer_store_dwordx4 v[74:77], v156, s[16:19], s67 offen offset:0 sc1\n"
  "v_exp_f32 v53, v53\n"
  "v_exp_f32 v54, v54\n"
  "s_waitcnt lgkmcnt(0)\n"
  "v_smfmac_f32_32x32x32_bf16 v[16:31], v[84:87], v[32:39], v157\n"
  "v_exp_f32 v55, v55\n"
  "v_exp_f32 v56, v56\n"
  "v_exp_f32 v57, v57\n"
  "v_smfmac_f32_32x32x32_bf16 v[16:31], v[88:91], v[40:47], v157\n"
  "v_exp_f32 v58, v58\n"
  "v_exp_f32 v59, v59\n"
  "v_exp_f32 v60, v60\n"
  "buffer_store_dwordx4 v[108:111], v156, s[16:19], s65 offen offset:1024 sc1\n"
  "v_exp_f32 v61, v61\n"
  "v_exp_f32 v62, v62\n"
  "v_exp_f32 v63, v63\n"
  "v_mfma_f32_32x32x16_bf16 v[32:47], v[136:139], v[144:147], 0\n"
  "ds_read_b128 v[100:103], v158 offset:1024\n"
  "s_setprio 1\n"
  "v_exp_f32 v120, v120\n"
  "v_exp_f32 v121, v121\n"
  "v_exp_f32 v122, v122\n"
  "v_mfma_f32_32x32x8_bf16 v[32:47], v[142:143], v[148:149], v[32:47]\n"
  "ds_read_b64 v[140:141], v160 offset:0\n"
  "v_exp_f32 v123, v123\n"
  "v_exp_f32 v124, v124\n"
  "v_exp_f32 v125, v125\n"
  "v_exp_f32 v126, v126\n"
  "v_smfmac_f32_32x32x32_bf16 v[0:15], v[84:87], v[48:55], v157\n"
  "ds_read_b64_tr_b16 v[92:93], v68 offset:0\n"
  "ds_read_b64_tr_b16 v[94:95], v69 offset:0\n"
  "v_exp_f32 v127, v127\n"
  "v_exp_f32 v128, v128\n"
  "v_exp_f32 v129, v129\n"
  "v_smfmac_f32_32x32x32_bf16 v[0:15], v[88:91], v[56:63], v157\n"
  "ds_read_b64_tr_b16 v[96:97], v70 offset:0\n"
  "ds_read_b64_tr_b16 v[98:99], v71 offset:0\n"
  "v_exp_f32 v130, v130\n"
  "v_exp_f32 v131, v131\n"
  "v_exp_f32 v132, v132\n"
  "buffer_store_dwordx4 v[112:115], v156, s[16:19], s66 offen offset:0 sc1\n"
  "v_exp_f32 v133, v133\n"
  "v_exp_f32 v134, v134\n"
  "v_exp_f32 v135, v135\n"
  "s_waitcnt lgkmcnt(4)\n"
  "v_mfma_f32_32x32x16_bf16 v[48:63], v[100:103], v[150:153], 0\n"
  "s_setprio 0\n"
  "v_exp_f32 v32, v32\n"
  "v_exp_f32 v33, v33\n"
  "v_exp_f32 v34, v34\n"
  "v_mfma_f32_32x32x8_bf16 v[48:63], v[140:141], v[154:155], v[48:63]\n"
  "v_exp_f32 v35, v35\n"
  "v_exp_f32 v36, v36\n"
  "buffer_store_dwordx4 v[78:81], v156, s[16:19], s67 offen offset:1024 sc1\n"
  "v_exp_f32 v37, v37\n"
  "v_exp_f32 v38, v38\n"
  "s_waitcnt lgkmcnt(0)\n"
  "v_smfmac_f32_32x32x32_bf16 v[16:31], v[92:95], v[120:127], v157\n"
  "v_exp_f32 v39, v39\n"
  "v_exp_f32 v40, v40\n"
  "v_exp_f32 v41, v41\n"
  "v_smfmac_f32_32x32x32_bf16 v[16:31], v[96:99], v[128:135], v157\n"
  "v_exp_f32 v42, v42\n"
  "v_exp_f32 v43, v43\n"
  "v_exp_f32 v44, v44\n"
  "buffer_store_dwordx4 v[116:119], v156, s[16:19], s66 offen offset:1024 sc1\n"
  "v_exp_f32 v45, v45\n"
  "v_exp_f32 v46, v46\n"
  "v_exp_f32 v47, v47\n"
  "s_barrier\n"
  "v_mfma_f32_32x32x16_bf16 v[120:135], v[100:103], v[144:147], 0\n"
  "ds_read_b128 v[136:139], v158 offset:12352\n"
  "s_setprio 3\n"
  "v_exp_f32 v48, v48\n"
  "v_exp_f32 v49, v49\n"
  "v_exp_f32 v50, v50\n"
  "v_mfma_f32_32x32x8_bf16 v[120:135], v[140:141], v[148:149], v[120:135]\n"
  "ds_read_b64 v[142:143], v159 offset:12352\n"
  "v_exp_f32 v51, v51\n"
  "v_exp_f32 v52, v52\n"
  "v_exp_f32 v53, v53\n"
  "v_exp_f32 v54, v54\n"
  "v_smfmac_f32_32x32x32_bf16 v[0:15], v[92:95], v[32:39], v157\n"
  "ds_read_b64_tr_b16 v[84:85], v64 offset:12352\n"
  "ds_read_b64_tr_b16 v[86:87], v65 offset:12352\n"
  "v_exp_f32 v55, v55\n"
  "v_exp_f32 v56, v56\n"
  "v_exp_f32 v57, v57\n"
  "v_smfmac_f32_32x32x32_bf16 v[0:15], v[96:99], v[40:47], v157\n"
  "ds_read_b64_tr_b16 v[88:89], v66 offset:12352\n"
  "ds_read_b64_tr_b16 v[90:91], v67 offset:12352\n"
  "v_exp_f32 v58, v58\n"
  "v_exp_f32 v59, v59\n"
  "v_exp_f32 v60, v60\n"
  "v_exp_f32 v61, v61\n"
  "v_exp_f32 v62, v62\n"
  "v_exp_f32 v63, v63\n"
  "s_waitcnt lgkmcnt(4)\n"
  "v_mfma_f32_32x32x16_bf16 v[32:47], v[136:139], v[150:153], 0\n"
  "s_setprio 2\n"
  "v_exp_f32 v120, v120\n"
  "v_exp_f32 v121, v121\n"
  "v_exp_f32 v122, v122\n"
  "v_mfma_f32_32x32x8_bf16 v[32:47], v[142:143], v[154:155], v[32:47]\n"
  "v_exp_f32 v123, v123\n"
  "v_exp_f32 v124, v124\n"
  "v_exp_f32 v125, v125\n"
  "v_exp_f32 v126, v126\n"
  "s_waitcnt lgkmcnt(0)\n"
  "v_smfmac_f32_32x32x32_bf16 v[16:31], v[84:87], v[48:55], v157\n"
  "v_exp_f32 v127, v127\n"
  "v_exp_f32 v128, v128\n"
  "v_exp_f32 v129, v129\n"
  "v_smfmac_f32_32x32x32_bf16 v[16:31], v[88:91], v[56:63], v157\n"
  "v_exp_f32 v130, v130\n"
  "v_exp_f32 v131, v131\n"
  "v_exp_f32 v132, v132\n"
  "v_exp_f32 v133, v133\n"
  "v_exp_f32 v134, v134\n"
  "v_exp_f32 v135, v135\n"
  "v_mfma_f32_32x32x16_bf16 v[48:63], v[136:139], v[144:147], 0\n"
  "ds_read_b128 v[100:103], v158 offset:13376\n"
  "s_setprio 1\n"
  "v_exp_f32 v32, v32\n"
  "v_exp_f32 v33, v33\n"
  "v_exp_f32 v34, v34\n"
  "v_mfma_f32_32x32x8_bf16 v[48:63], v[142:143], v[148:149], v[48:63]\n"
  "ds_read_b64 v[140:141], v160 offset:12352\n"
  "v_exp_f32 v35, v35\n"
  "v_exp_f32 v36, v36\n"
  "v_exp_f32 v37, v37\n"
  "v_exp_f32 v38, v38\n"
  "v_smfmac_f32_32x32x32_bf16 v[0:15], v[84:87], v[120:127], v157\n"
  "ds_read_b64_tr_b16 v[92:93], v68 offset:12352\n"
  "ds_read_b64_tr_b16 v[94:95], v69 offset:12352\n"
  "v_exp_f32 v39, v39\n"
  "v_exp_f32 v40, v40\n"
  "v_exp_f32 v41, v41\n"
  "v_smfmac_f32_32x32x32_bf16 v[0:15], v[88:91], v[128:135], v157\n"
  "ds_read_b64_tr_b16 v[96:97], v70 offset:12352\n"
  "ds_read_b64_tr_b16 v[98:99], v71 offset:12352\n"
  "v_exp_f32 v42, v42\n"
  "v_exp_f32 v43, v43\n"
  "v_exp_f32 v44, v44\n"
  "v_exp_f32 v45, v45\n"
  "v_exp_f32 v46, v46\n"
  "v_exp_f32 v47, v47\n"
  "s_waitcnt lgkmcnt(4)\n"
  "v_mfma_f32_32x32x16_bf16 v[120:135], v[100:103], v[150:153], 0\n"
  "s_setprio 0\n"
  "v_exp_f32 v48, v48\n"
  "v_exp_f32 v49, v49\n"
  "v_exp_f32 v50, v50\n"
  "v_mfma_f32_32x32x8_bf16 v[120:135], v[140:141], v[154:155], v[120:135]\n"
  "v_exp_f32 v51, v51\n"
  "v_exp_f32 v52, v52\n"
  "v_exp_f32 v53, v53\n"
  "v_exp_f32 v54, v54\n"
  "s_waitcnt lgkmcnt(0)\n"
  "v_smfmac_f32_32x32x32_bf16 v[16:31], v[92:95], v[32:39], v157\n"
  "v_exp_f32 v55, v55\n"
  "v_exp_f32 v56, v56\n"
  "v_exp_f32 v57, v57\n"
  "v_smfmac_f32_32x32x32_bf16 v[16:31], v[96:99], v[40:47], v157\n"
  "v_exp_f32 v58, v58\n"
  "v_exp_f32 v59, v59\n"
  "v_exp_f32 v60, v60\n"
  "v_exp_f32 v61, v61\n"
  "v_exp_f32 v62, v62\n"
  "v_exp_f32 v63, v63\n"
  "s_waitcnt vmcnt(0)\n"
  "s_barrier\n"
  "v_mfma_f32_32x32x16_bf16 v[32:47], v[100:103], v[144:147], 0\n"
  "ds_read_b128 v[136:139], v158 offset:24704\n"
  "s_setprio 3\n"
  "v_exp_f32 v120, v120\n"
  "v_exp_f32 v121, v121\n"
  "v_exp_f32 v122, v122\n"
  "v_mfma_f32_32x32x8_bf16 v[32:47], v[140:141], v[148:149], v[32:47]\n"
  "ds_read_b64 v[142:143], v159 offset:24704\n"
  "v_exp_f32 v123, v123\n"
  "v_exp_f32 v124, v124\n"
  "v_exp_f32 v125, v125\n"
  "v_exp_f32 v126, v126\n"
  "v_smfmac_f32_32x32x32_bf16 v[0:15], v[92:95], v[48:55], v157\n"
  "ds_read_b64_tr_b16 v[84:85], v64 offset:24704\n"
  "ds_read_b64_tr_b16 v[86:87], v65 offset:24704\n"
  "v_exp_f32 v127, v127\n"
  "v_exp_f32 v128, v128\n"
  "v_exp_f32 v129, v129\n"
  "v_smfmac_f32_32x32x32_bf16 v[0:15], v[96:99], v[56:63], v157\n"
  "ds_read_b64_tr_b16 v[88:89], v66 offset:24704\n"
  "ds_read_b64_tr_b16 v[90:91], v67 offset:24704\n"
  "v_exp_f32 v130, v130\n"
  "v_exp_f32 v131, v131\n"
  "v_exp_f32 v132, v132\n"
  "v_exp_f32 v133, v133\n"
  "v_exp_f32 v134, v134\n"
  "v_exp_f32 v135, v135\n"
  "s_waitcnt lgkmcnt(4)\n"
  "v_mfma_f32_32x32x16_bf16 v[48:63], v[136:139], v[150:153], 0\n"
  "s_setprio 2\n"
  "v_exp_f32 v32, v32\n"
  "v_exp_f32 v33, v33\n"
  "v_exp_f32 v34, v34\n"
  "v_mfma_f32_32x32x8_bf16 v[48:63], v[142:143], v[154:155], v[48:63]\n"
  "v_exp_f32 v35, v35\n"
  "v_exp_f32 v36, v36\n"
  "v_exp_f32 v37, v37\n"
  "v_exp_f32 v38, v38\n"
  "s_waitcnt lgkmcnt(0)\n"
  "v_smfmac_f32_32x32x32_bf16 v[16:31], v[84:87], v[120:127], v157\n"
  "v_exp_f32 v39, v39\n"
  "v_exp_f32 v40, v40\n"
  "v_exp_f32 v41, v41\n"
  "v_smfmac_f32_32x32x32_bf16 v[16:31], v[88:91], v[128:135], v157\n"
  "v_exp_f32 v42, v42\n"
  "v_exp_f32 v43, v43\n"
  "v_exp_f32 v44, v44\n"
  "v_exp_f32 v45, v45\n"
  "v_exp_f32 v46, v46\n"
  "v_exp_f32 v47, v47\n"
  "v_mfma_f32_32x32x16_bf16 v[120:135], v[136:139], v[144:147], 0\n"
  "ds_read_b128 v[100:103], v158 offset:25728\n"
  "s_setprio 1\n"
  "v_exp_f32 v48, v48\n"
  "v_exp_f32 v49, v49\n"
  "v_exp_f32 v50, v50\n"
  "v_mfma_f32_32x32x8_bf16 v[120:135], v[142:143], v[148:149], v[120:135]\n"
  "ds_read_b64 v[140:141], v160 offset:24704\n"
  "v_exp_f32 v51, v51\n"
  "v_exp_f32 v52, v52\n"
  "v_exp_f32 v53, v53\n"
  "v_exp_f32 v54, v54\n"
  "v_smfmac_f32_32x32x32_bf16 v[0:15], v[84:87], v[32:39], v157\n"
  "ds_read_b64_tr_b16 v[92:93], v68 offset:24704\n"
  "ds_read_b64_tr_b16 v[94:95], v69 offset:24704\n"
  "v_exp_f32 v55, v55\n"
  "v_exp_f32 v56, v56\n"
  "v_exp_f32 v57, v57\n"
  "v_smfmac_f32_32x32x32_bf16 v[0:15], v[88:91], v[40:47], v157\n"
  "ds_read_b64_tr_b16 v[96:97], v70 offset:24704\n"
  "ds_read_b64_tr_b16 v[98:99], v71 offset:24704\n"
  "v_exp_f32 v58, v58\n"
  "v_exp_f32 v59, v59\n"
  "v_exp_f32 v60, v60\n"
  "v_exp_f32 v61, v61\n"
  "v_exp_f32 v62, v62\n"
  "v_exp_f32 v63, v63\n"
  "s_waitcnt lgkmcnt(4)\n"
  "v_mfma_f32_32x32x16_bf16 v[32:47], v[100:103], v[150:153], 0\n"
  "s_setprio 0\n"
  "v_exp_f32 v120, v120\n"
  "v_exp_f32 v121, v121\n"
  "v_exp_f32 v122, v122\n"
  "v_mfma_f32_32x32x8_bf16 v[32:47], v[140:141], v[154:155], v[32:47]\n"
  "v_exp_f32 v123, v123\n"
  "v_exp_f32 v124, v124\n"
  "v_exp_f32 v125, v125\n"
  "v_exp_f32 v126, v126\n"
  "s_waitcnt lgkmcnt(0)\n"
  "v_smfmac_f32_32x32x32_bf16 v[16:31], v[92:95], v[48:55], v157\n"
  "v_exp_f32 v127, v127\n"
  "v_exp_f32 v128, v128\n"
  "v_exp_f32 v129, v129\n"
  "v_smfmac_f32_32x32x32_bf16 v[16:31], v[96:99], v[56:63], v157\n"
  "v_exp_f32 v130, v130\n"
  "v_exp_f32 v131, v131\n"
  "v_exp_f32 v132, v132\n"
  "v_exp_f32 v133, v133\n"
  "v_exp_f32 v134, v134\n"
  "v_exp_f32 v135, v135\n"
  "s_barrier\n"
  "v_mfma_f32_32x32x16_bf16 v[48:63], v[100:103], v[144:147], 0\n"
  "ds_read_b128 v[136:139], v158 offset:0\n"
  "s_setprio 3\n"
  "v_exp_f32 v32, v32\n"
  "v_exp_f32 v33, v33\n"
  "v_exp_f32 v34, v34\n"
  "v_mfma_f32_32x32x8_bf16 v[48:63], v[140:141], v[148:149], v[48:63]\n"
  "ds_read_b64 v[142:143], v159 offset:0\n"
  "v_exp_f32 v35, v35\n"
  "v_exp_f32 v36, v36\n"
  "v_exp_f32 v37, v37\n"
  "v_exp_f32 v38, v38\n"
  "v_smfmac_f32_32x32x32_bf16 v[0:15], v[92:95], v[120:127], v157\n"
  "ds_read_b64_tr_b16 v[84:85], v64 offset:0\n"
  "ds_read_b64_tr_b16 v[86:87], v65 offset:0\n"
  "v_exp_f32 v39, v39\n"
  "v_exp_f32 v40, v40\n"
  "v_exp_f32 v41, v41\n"
  "v_smfmac_f32_32x32x32_bf16 v[0:15], v[96:99], v[128:135], v157\n"
  "ds_read_b64_tr_b16 v[88:89], v66 offset:0\n"
  "ds_read_b64_tr_b16 v[90:91], v67 offset:0\n"
  "v_exp_f32 v42, v42\n"
  "v_exp_f32 v43, v43\n"
  "v_exp_f32 v44, v44\n"
  "v_exp_f32 v45, v45\n"
  "v_exp_f32 v46, v46\n"
  "v_exp_f32 v47, v47\n"
  "s_waitcnt lgkmcnt(4)\n"
  "v_mfma_f32_32x32x16_bf16 v[120:135], v[136:139], v[150:153], 0\n"
  "s_setprio 2\n"
  "v_exp_f32 v48, v48\n"
  "v_exp_f32 v49, v49\n"
  "v_exp_f32 v50, v50\n"
  "v_mfma_f32_32x32x8_bf16 v[120:135], v[142:143], v[154:155], v[120:135]\n"
  "v_exp_f32 v51, v51\n"
  "v_exp_f32 v52, v52\n"
  "v_exp_f32 v53, v53\n"
  "v_exp_f32 v54, v54\n"
  "s_waitcnt lgkmcnt(0)\n"
  "v_smfmac_f32_32x32x32_bf16 v[16:31], v[84:87], v[32:39], v157\n"
  "v_exp_f32 v55, v55\n"
  "v_exp_f32 v56, v56\n"
  "v_exp_f32 v57, v57\n"
  "v_smfmac_f32_32x32x32_bf16 v[16:31], v[88:91], v[40:47], v157\n"
  "v_exp_f32 v58, v58\n"
  "v_exp_f32 v59, v59\n"
  "v_exp_f32 v60, v60\n"
  "v_exp_f32 v61, v61\n"
  "v_exp_f32 v62, v62\n"
  "v_exp_f32 v63, v63\n"
  "v_mfma_f32_32x32x16_bf16 v[32:47], v[136:139], v[144:147], 0\n"
  "ds_read_b128 v[100:103], v158 offset:1024\n"
  "s_setprio 1\n"
  "v_exp_f32 v120, v120\n"
  "v_exp_f32 v121, v121\n"
  "v_exp_f32 v122, v122\n"
  "v_mfma_f32_32x32x8_bf16 v[32:47], v[142:143], v[148:149], v[32:47]\n"
  "ds_read_b64 v[140:141], v160 offset:0\n"
  "v_exp_f32 v123, v123\n"
  "v_exp_f32 v124, v124\n"
  "v_exp_f32 v125, v125\n"
  "v_exp_f32 v126, v126\n"
  "v_smfmac_f32_32x32x32_bf16 v[0:15], v[84:87], v[48:55], v157\n"
  "ds_read_b64_tr_b16 v[92:93], v68 offset:0\n"
  "ds_read_b64_tr_b16 v[94:95], v69 offset:0\n"
  "v_exp_f32 v127, v127\n"
  "v_exp_f32 v128, v128\n"
  "v_exp_f32 v129, v129\n"
  "v_smfmac_f32_32x32x32_bf16 v[0:15], v[88:91], v[56:63], v157\n"
  "ds_read_b64_tr_b16 v[96:97], v70 offset:0\n"
  "ds_read_b64_tr_b16 v[98:99], v71 offset:0\n"
  "v_exp_f32 v130, v130\n"
  "v_exp_f32 v131, v131\n"
  "v_exp_f32 v132, v132\n"
  "v_exp_f32 v133, v133\n"
  "v_exp_f32 v134, v134\n"
  "v_exp_f32 v135, v135\n"
  "s_waitcnt lgkmcnt(4)\n"
  "v_mfma_f32_32x32x16_bf16 v[48:63], v[100:103], v[150:153], 0\n"
  "s_setprio 0\n"
  "v_exp_f32 v32, v32\n"
  "v_exp_f32 v33, v33\n"
  "v_exp_f32 v34, v34\n"
  "v_mfma_f32_32x32x8_bf16 v[48:63], v[140:141], v[154:155], v[48:63]\n"
  "v_exp_f32 v35, v35\n"
  "v_exp_f32 v36, v36\n"
  "v_exp_f32 v37, v37\n"
  "v_exp_f32 v38, v38\n"
  "s_waitcnt lgkmcnt(0)\n"
  "v_smfmac_f32_32x32x32_bf16 v[16:31], v[92:95], v[120:127], v157\n"
  "v_exp_f32 v39, v39\n"
  "v_exp_f32 v40, v40\n"
  "v_exp_f32 v41, v41\n"
  "v_smfmac_f32_32x32x32_bf16 v[16:31], v[96:99], v[128:135], v157\n"
  "v_exp_f32 v42, v42\n"
  "v_exp_f32 v43, v43\n"
  "v_exp_f32 v44, v44\n"
  "v_exp_f32 v45, v45\n"
  "v_exp_f32 v46, v46\n"
  "v_exp_f32 v47, v47\n"
  "s_barrier\n"
  "v_mfma_f32_32x32x16_bf16 v[120:135], v[100:103], v[144:147], 0\n"
  "ds_read_b128 v[136:139], v158 offset:12352\n"
  "s_setprio 3\n"
  "v_exp_f32 v48, v48\n"
  "v_exp_f32 v49, v49\n"
  "v_exp_f32 v50, v50\n"
  "v_mfma_f32_32x32x8_bf16 v[120:135], v[140:141], v[148:149], v[120:135]\n"
  "ds_read_b64 v[142:143], v159 offset:12352\n"
  "v_exp_f32 v51, v51\n"
  "v_exp_f32 v52, v52\n"
  "v_exp_f32 v53, v53\n"
  "v_exp_f32 v54, v54\n"
  "v_smfmac_f32_32x32x32_bf16 v[0:15], v[92:95], v[32:39], v157\n"
  "ds_read_b64_tr_b16 v[84:85], v64 offset:12352\n"
  "ds_read_b64_tr_b16 v[86:87], v65 offset:12352\n"
  "v_exp_f32 v55, v55\n"
  "v_exp_f32 v56, v56\n"
  "v_exp_f32 v57, v57\n"
  "v_smfmac_f32_32x32x32_bf16 v[0:15], v[96:99], v[40:47], v157\n"
  "ds_read_b64_tr_b16 v[88:89], v66 offset:12352\n"
  "ds_read_b64_tr_b16 v[90:91], v67 offset:12352\n"
  "v_exp_f32 v58, v58\n"
  "v_exp_f32 v59, v59\n"
  "v_exp_f32 v60, v60\n"
  "v_exp_f32 v61, v61\n"
  "v_exp_f32 v62, v62\n"
  "v_exp_f32 v63, v63\n"
  "s_waitcnt lgkmcnt(4)\n"
  "v_mfma_f32_32x32x16_bf16 v[32:47], v[136:139], v[150:153], 0\n"
  "s_setprio 2\n"
  "v_exp_f32 v120, v120\n"
  "v_exp_f32 v121, v121\n"
  "v_exp_f32 v122, v122\n"
  "v_mfma_f32_32x32x8_bf16 v[32:47], v[142:143], v[154:155], v[32:47]\n"
  "v_exp_f32 v123, v123\n"
  "v_exp_f32 v124, v124\n"
  "v_exp_f32 v125, v125\n"
  "v_exp_f32 v126, v126\n"
  "s_waitcnt lgkmcnt(0)\n"
  "v_smfmac_f32_32x32x32_bf16 v[16:31], v[84:87], v[48:55], v157\n"
  "v_exp_f32 v127, v127\n"
  "v_exp_f32 v128, v128\n"
  "v_exp_f32 v129, v129\n"
  "v_smfmac_f32_32x32x32_bf16 v[16:31], v[88:91], v[56:63], v157\n"
  "v_exp_f32 v130, v130\n"
  "v_exp_f32 v131, v131\n"
  "v_exp_f32 v132, v132\n"
  "v_exp_f32 v133, v133\n"
  "v_exp_f32 v134, v134\n"
  "v_exp_f32 v135, v135\n"
  "v_mfma_f32_32x32x16_bf16 v[48:63], v[136:139], v[144:147], 0\n"
  "ds_read_b128 v[100:103], v158 offset:13376\n"
  "s_setprio 1\n"
  "v_exp_f32 v32, v32\n"
  "v_exp_f32 v33, v33\n"
  "v_exp_f32 v34, v34\n"
  "v_mfma_f32_32x32x8_bf16 v[48:63], v[142:143], v[148:149], v[48:63]\n"
  "ds_read_b64 v[140:141], v160 offset:12352\n"
  "v_exp_f32 v35, v35\n"
  "v_exp_f32 v36, v36\n"
  "v_exp_f32 v37, v37\n"
  "v_exp_f32 v38, v38\n"
  "v_smfmac_f32_32x32x32_bf16 v[0:15], v[84:87], v[120:127], v157\n"
  "ds_read_b64_tr_b16 v[92:93], v68 offset:12352\n"
  "ds_read_b64_tr_b16 v[94:95], v69 offset:12352\n"
  "v_exp_f32 v39, v39\n"
  "v_exp_f32 v40, v40\n"
  "v_exp_f32 v41, v41\n"
  "v_smfmac_f32_32x32x32_bf16 v[0:15], v[88:91], v[128:135], v157\n"
  "ds_read_b64_tr_b16 v[96:97], v70 offset:12352\n"
  "ds_read_b64_tr_b16 v[98:99], v71 offset:12352\n"
  "v_exp_f32 v42, v42\n"
  "v_exp_f32 v43, v43\n"
  "v_exp_f32 v44, v44\n"
  "v_exp_f32 v45, v45\n"
  "v_exp_f32 v46, v46\n"
  "v_exp_f32 v47, v47\n"
  "s_waitcnt lgkmcnt(4)\n"
  "v_mfma_f32_32x32x16_bf16 v[120:135], v[100:103], v[150:153], 0\n"
  "s_setprio 0\n"
  "v_exp_f32 v48, v48\n"
  "v_exp_f32 v49, v49\n"
  "v_exp_f32 v50, v50\n"
  "v_mfma_f32_32x32x8_bf16 v[120:135], v[140:141], v[154:155], v[120:135]\n"
  "v_exp_f32 v51, v51\n"
  "v_exp_f32 v52, v52\n"
  "v_exp_f32 v53, v53\n"
  "v_exp_f32 v54, v54\n"
  "s_waitcnt lgkmcnt(0)\n"
  "v_smfmac_f32_32x32x32_bf16 v[16:31], v[92:95], v[32:39], v157\n"
  "v_exp_f32 v55, v55\n"
  "v_exp_f32 v56, v56\n"
  "v_exp_f32 v57, v57\n"
  "v_smfmac_f32_32x32x32_bf16 v[16:31], v[96:99], v[40:47], v157\n"
  "v_exp_f32 v58, v58\n"
  "v_exp_f32 v59, v59\n"
  "v_exp_f32 v60, v60\n"
  "v_exp_f32 v61, v61\n"
  "v_exp_f32 v62, v62\n"
  "v_exp_f32 v63, v63\n"
  "s_barrier\n"
  "v_mfma_f32_32x32x16_bf16 v[32:47], v[100:103], v[144:147], 0\n"
  "ds_read_b128 v[136:139], v158 offset:24704\n"
  "s_setprio 3\n"
  "v_exp_f32 v120, v120\n"
  "v_exp_f32 v121, v121\n"
  "v_exp_f32 v122, v122\n"
  "v_mfma_f32_32x32x8_bf16 v[32:47], v[140:141], v[148:149], v[32:47]\n"
  "ds_read_b64 v[142:143], v159 offset:24704\n"
  "v_exp_f32 v123, v123\n"
  "v_exp_f32 v124, v124\n"
  "v_exp_f32 v125, v125\n"
  "v_exp_f32 v126, v126\n"
  "v_smfmac_f32_32x32x32_bf16 v[0:15], v[92:95], v[48:55], v157\n"
  "ds_read_b64_tr_b16 v[84:85], v64 offset:24704\n"
  "ds_read_b64_tr_b16 v[86:87], v65 offset:24704\n"
  "v_exp_f32 v127, v127\n"
  "v_exp_f32 v128, v128\n"
  "v_exp_f32 v129, v129\n"
  "v_smfmac_f32_32x32x32_bf16 v[0:15], v[96:99], v[56:63], v157\n"
  "ds_read_b64_tr_b16 v[88:89], v66 offset:24704\n"
  "ds_read_b64_tr_b16 v[90:91], v67 offset:24704\n"
  "v_exp_f32 v130, v130\n"
  "v_exp_f32 v131, v131\n"
  "v_exp_f32 v132, v132\n"
  "v_exp_f32 v133, v133\n"
  "v_exp_f32 v134, v134\n"
  "v_exp_f32 v135, v135\n"
  "s_waitcnt lgkmcnt(4)\n"
  "v_mfma_f32_32x32x16_bf16 v[48:63], v[136:139], v[150:153], 0\n"
  "s_setprio 2\n"
  "v_exp_f32 v32, v32\n"
  "v_exp_f32 v33, v33\n"
  "v_exp_f32 v34, v34\n"
  "v_mfma_f32_32x32x8_bf16 v[48:63], v[142:143], v[154:155], v[48:63]\n"
  "v_exp_f32 v35, v35\n"
  "v_exp_f32 v36, v36\n"
  "v_exp_f32 v37, v37\n"
  "v_exp_f32 v38, v38\n"
  "s_waitcnt lgkmcnt(0)\n"
  "v_smfmac_f32_32x32x32_bf16 v[16:31], v[84:87], v[120:127], v157\n"
  "v_exp_f32 v39, v39\n"
  "v_exp_f32 v40, v40\n"
  "v_exp_f32 v41, v41\n"
  "v_smfmac_f32_32x32x32_bf16 v[16:31], v[88:91], v[128:135], v157\n"
  "v_exp_f32 v42, v42\n"
  "v_exp_f32 v43, v43\n"
  "v_exp_f32 v44, v44\n"
  "v_exp_f32 v45, v45\n"
  "v_exp_f32 v46, v46\n"
  "v_exp_f32 v47, v47\n"
  "v_mfma_f32_32x32x16_bf16 v[120:135], v[136:139], v[144:147], 0\n"
  "ds_read_b128 v[100:103], v158 offset:25728\n"
  "s_setprio 1\n"
  "v_exp_f32 v48, v48\n"
  "v_exp_f32 v49, v49\n"
  "v_exp_f32 v50, v50\n"
  "v_mfma_f32_32x32x8_bf16 v[120:135], v[142:143], v[148:149], v[120:135]\n"
  "ds_read_b64 v[140:141], v160 offset:24704\n"
  "v_exp_f32 v51, v51\n"
  "v_exp_f32 v52, v52\n"
  "v_exp_f32 v53, v53\n"
  "v_exp_f32 v54, v54\n"
  "v_smfmac_f32_32x32x32_bf16 v[0:15], v[84:87], v[32:39], v157\n"
  "ds_read_b64_tr_b16 v[92:93], v68 offset:24704\n"
  "ds_read_b64_tr_b16 v[94:95], v69 offset:24704\n"
  "v_exp_f32 v55, v55\n"
  "v_exp_f32 v56, v56\n"
  "v_exp_f32 v57, v57\n"
  "v_smfmac_f32_32x32x32_bf16 v[0:15], v[88:91], v[40:47], v157\n"
  "ds_read_b64_tr_b16 v[96:97], v70 offset:24704\n"
  "ds_read_b64_tr_b16 v[98:99], v71 offset:24704\n"
  "v_exp_f32 v58, v58\n"
  "v_exp_f32 v59, v59\n"
  "v_exp_f32 v60, v60\n"
  "v_exp_f32 v61, v61\n"
  "v_exp_f32 v62, v62\n"
  "v_exp_f32 v63, v63\n"
  "s_waitcnt lgkmcnt(4)\n"
  "v_mfma_f32_32x32x16_bf16 v[32:47], v[100:103], v[150:153], 0\n"
  "s_setprio 0\n"
  "v_exp_f32 v120, v120\n"
  "v_exp_f32 v121, v121\n"
  "v_exp_f32 v122, v122\n"
  "v_mfma_f32_32x32x8_bf16 v[32:47], v[140:141], v[154:155], v[32:47]\n"
  "v_exp_f32 v123, v123\n"
  "v_exp_f32 v124, v124\n"
  "v_exp_f32 v125, v125\n"
  "v_exp_f32 v126, v126\n"
  "s_waitcnt lgkmcnt(0)\n"
  "v_smfmac_f32_32x32x32_bf16 v[16:31], v[92:95], v[48:55], v157\n"
  "v_exp_f32 v127, v127\n"
  "v_exp_f32 v128, v128\n"
  "v_exp_f32 v129, v129\n"
  "v_smfmac_f32_32x32x32_bf16 v[16:31], v[96:99], v[56:63], v157\n"
  "v_exp_f32 v130, v130\n"
  "v_exp_f32 v131, v131\n"
  "v_exp_f32 v132, v132\n"
  "v_exp_f32 v133, v133\n"
  "v_exp_f32 v134, v134\n"
  "v_exp_f32 v135, v135\n"
  "s_barrier\n"
  "v_mfma_f32_32x32x16_bf16 v[48:63], v[100:103], v[144:147], 0\n"
  "ds_read_b128 v[136:139], v158 offset:0\n"
  "s_setprio 3\n"
  "v_exp_f32 v32, v32\n"
  "v_exp_f32 v33, v33\n"
  "v_exp_f32 v34, v34\n"
  "v_mfma_f32_32x32x8_bf16 v[48:63], v[140:141], v[148:149], v[48:63]\n"
  "ds_read_b64 v[142:143], v159 offset:0\n"
  "v_exp_f32 v35, v35\n"
  "v_exp_f32 v36, v36\n"
  "v_exp_f32 v37, v37\n"
  "v_exp_f32 v38, v38\n"
  "v_smfmac_f32_32x32x32_bf16 v[0:15], v[92:95], v[120:127], v157\n"
  "ds_read_b64_tr_b16 v[84:85], v64 offset:0\n"
  "ds_read_b64_tr_b16 v[86:87], v65 offset:0\n"
  "v_exp_f32 v39, v39\n"
  "v_exp_f32 v40, v40\n"
  "v_exp_f32 v41, v41\n"
  "v_smfmac_f32_32x32x32_bf16 v[0:15], v[96:99], v[128:135], v157\n"
  "ds_read_b64_tr_b16 v[88:89], v66 offset:0\n"
  "ds_read_b64_tr_b16 v[90:91], v67 offset:0\n"
  "v_exp_f32 v42, v42\n"
  "v_exp_f32 v43, v43\n"
  "v_exp_f32 v44, v44\n"
  "v_exp_f32 v45, v45\n"
  "v_exp_f32 v46, v46\n"
  "v_exp_f32 v47, v47\n"
  "s_waitcnt lgkmcnt(4)\n"
  "v_mfma_f32_32x32x16_bf16 v[120:135], v[136:139], v[150:153], 0\n"
  "s_setprio 2\n"
  "v_exp_f32 v48, v48\n"
  "v_exp_f32 v49, v49\n"
  "v_exp_f32 v50, v50\n"
  "v_mfma_f32_32x32x8_bf16 v[120:135], v[142:143], v[154:155], v[120:135]\n"
  "v_exp_f32 v51, v51\n"
  "v_exp_f32 v52, v52\n"
  "v_exp_f32 v53, v53\n"
  "v_exp_f32 v54, v54\n"
  "s_waitcnt lgkmcnt(0)\n"
  "v_smfmac_f32_32x32x32_bf16 v[16:31], v[84:87], v[32:39], v157\n"
  "v_exp_f32 v55, v55\n"
  "v_exp_f32 v56, v56\n"
  "v_exp_f32 v57, v57\n"
  "v_smfmac_f32_32x32x32_bf16 v[16:31], v[88:91], v[40:47], v157\n"
  "v_exp_f32 v58, v58\n"
  "v_exp_f32 v59, v59\n"
  "v_exp_f32 v60, v60\n"
  "v_exp_f32 v61, v61\n"
  "v_exp_f32 v62, v62\n"
  "v_exp_f32 v63, v63\n"
  "v_mfma_f32_32x32x16_bf16 v[32:47], v[136:139], v[144:147], 0\n"
  "ds_read_b128 v[100:103], v158 offset:1024\n"
  "s_setprio 1\n"
  "v_exp_f32 v120, v120\n"
  "v_exp_f32 v121, v121\n"
  "v_exp_f32 v122, v122\n"
  "v_mfma_f32_32x32x8_bf16 v[32:47], v[142:143], v[148:149], v[32:47]\n"
  "ds_read_b64 v[140:141], v160 offset:0\n"
  "v_exp_f32 v123, v123\n"
  "v_exp_f32 v124, v124\n"
  "v_exp_f32 v125, v125\n"
  "v_exp_f32 v126, v126\n"
  "v_smfmac_f32_32x32x32_bf16 v[0:15], v[84:87], v[48:55], v157\n"
  "ds_read_b64_tr_b16 v[92:93], v68 offset:0\n"
  "ds_read_b64_tr_b16 v[94:95], v69 offset:0\n"
  "v_exp_f32 v127, v127\n"
  "v_exp_f32 v128, v128\n"
  "v_exp_f32 v129, v129\n"
  "v_smfmac_f32_32x32x32_bf16 v[0:15], v[88:91], v[56:63], v157\n"
  "ds_read_b64_tr_b16 v[96:97], v70 offset:0\n"
  "ds_read_b64_tr_b16 v[98:99], v71 offset:0\n"
  "v_exp_f32 v130, v130\n"
  "v_exp_f32 v131, v131\n"
  "v_exp_f32 v132, v132\n"
  "v_exp_f32 v133, v133\n"
  "v_exp_f32 v134, v134\n"
  "v_exp_f32 v135, v135\n"
  "s_waitcnt lgkmcnt(4)\n"
  "v_mfma_f32_32x32x16_bf16 v[48:63], v[100:103], v[150:153], 0\n"
  "s_setprio 0\n"
  "v_exp_f32 v32, v32\n"
  "v_exp_f32 v33, v33\n"
  "v_exp_f32 v34, v34\n"
  "v_mfma_f32_32x32x8_bf16 v[48:63], v[140:141], v[154:155], v[48:63]\n"
  "v_exp_f32 v35, v35\n"
  "v_exp_f32 v36, v36\n"
  "v_exp_f32 v37, v37\n"
  "v_exp_f32 v38, v38\n"
  "s_waitcnt lgkmcnt(0)\n"
  "v_smfmac_f32_32x32x32_bf16 v[16:31], v[92:95], v[120:127], v157\n"
  "v_exp_f32 v39, v39\n"
  "v_exp_f32 v40, v40\n"
  "v_exp_f32 v41, v41\n"
  "v_smfmac_f32_32x32x32_bf16 v[16:31], v[96:99], v[128:135], v157\n"
  "v_exp_f32 v42, v42\n"
  "v_exp_f32 v43, v43\n"
  "v_exp_f32 v44, v44\n"
  "v_exp_f32 v45, v45\n"
  "v_exp_f32 v46, v46\n"
  "v_exp_f32 v47, v47\n"
  "s_barrier\n"
  "v_mfma_f32_32x32x16_bf16 v[120:135], v[100:103], v[144:147], 0\n"
  "ds_read_b128 v[136:139], v158 offset:12352\n"
  "s_setprio 3\n"
  "v_exp_f32 v48, v48\n"
  "v_exp_f32 v49, v49\n"
  "v_exp_f32 v50, v50\n"
  "v_mfma_f32_32x32x8_bf16 v[120:135], v[140:141], v[148:149], v[120:135]\n"
  "ds_read_b64 v[142:143], v159 offset:12352\n"
  "v_exp_f32 v51, v51\n"
  "v_exp_f32 v52, v52\n"
  "v_exp_f32 v53, v53\n"
  "v_exp_f32 v54, v54\n"
  "v_smfmac_f32_32x32x32_bf16 v[0:15], v[92:95], v[32:39], v157\n"
  "ds_read_b64_tr_b16 v[84:85], v64 offset:12352\n"
  "ds_read_b64_tr_b16 v[86:87], v65 offset:12352\n"
  "v_exp_f32 v55, v55\n"
  "v_exp_f32 v56, v56\n"
  "v_exp_f32 v57, v57\n"
  "v_smfmac_f32_32x32x32_bf16 v[0:15], v[96:99], v[40:47], v157\n"
  "ds_read_b64_tr_b16 v[88:89], v66 offset:12352\n"
  "ds_read_b64_tr_b16 v[90:91], v67 offset:12352\n"
  "v_exp_f32 v58, v58\n"
  "v_exp_f32 v59, v59\n"
  "v_exp_f32 v60, v60\n"
  "v_exp_f32 v61, v61\n"
  "v_exp_f32 v62, v62\n"
  "v_exp_f32 v63, v63\n"
  "s_waitcnt lgkmcnt(4)\n"
  "v_mfma_f32_32x32x16_bf16 v[32:47], v[136:139], v[150:153], 0\n"
  "s_setprio 2\n"
  "v_exp_f32 v120, v120\n"
  "v_exp_f32 v121, v121\n"
  "v_exp_f32 v122, v122\n"
  "v_mfma_f32_32x32x8_bf16 v[32:47], v[142:143], v[154:155], v[32:47]\n"
  "v_exp_f32 v123, v123\n"
  "v_exp_f32 v124, v124\n"
  "v_exp_f32 v125, v125\n"
  "v_exp_f32 v126, v126\n"
  "s_waitcnt lgkmcnt(0)\n"
  "v_smfmac_f32_32x32x32_bf16 v[16:31], v[84:87], v[48:55], v157\n"
  "v_exp_f32 v127, v127\n"
  "v_exp_f32 v128, v128\n"
  "v_exp_f32 v129, v129\n"
  "v_smfmac_f32_32x32x32_bf16 v[16:31], v[88:91], v[56:63], v157\n"
  "v_exp_f32 v130, v130\n"
  "v_exp_f32 v131, v131\n"
  "v_exp_f32 v132, v132\n"
  "v_exp_f32 v133, v133\n"
  "v_exp_f32 v134, v134\n"
  "v_exp_f32 v135, v135\n"
  "v_mfma_f32_32x32x16_bf16 v[48:63], v[136:139], v[144:147], 0\n"
  "ds_read_b128 v[100:103], v158 offset:13376\n"
  "s_setprio 1\n"
  "v_exp_f32 v32, v32\n"
  "v_exp_f32 v33, v33\n"
  "v_exp_f32 v34, v34\n"
  "v_mfma_f32_32x32x8_bf16 v[48:63], v[142:143], v[148:149], v[48:63]\n"
  "ds_read_b64 v[140:141], v160 offset:12352\n"
  "v_exp_f32 v35, v35\n"
  "v_exp_f32 v36, v36\n"
  "v_exp_f32 v37, v37\n"
  "v_exp_f32 v38, v38\n"
  "v_smfmac_f32_32x32x32_bf16 v[0:15], v[84:87], v[120:127], v157\n"
  "ds_read_b64_tr_b16 v[92:93], v68 offset:12352\n"
  "ds_read_b64_tr_b16 v[94:95], v69 offset:12352\n"
  "v_exp_f32 v39, v39\n"
  "v_exp_f32 v40, v40\n"
  "v_exp_f32 v41, v41\n"
  "v_smfmac_f32_32x32x32_bf16 v[0:15], v[88:91], v[128:135], v157\n"
  "ds_read_b64_tr_b16 v[96:97], v70 offset:12352\n"
  "ds_read_b64_tr_b16 v[98:99], v71 offset:12352\n"
  "v_exp_f32 v42, v42\n"
  "v_exp_f32 v43, v43\n"
  "v_exp_f32 v44, v44\n"
  "v_exp_f32 v45, v45\n"
  "v_exp_f32 v46, v46\n"
  "v_exp_f32 v47, v47\n"
  "s_waitcnt lgkmcnt(4)\n"
  "v_mfma_f32_32x32x16_bf16 v[120:135], v[100:103], v[150:153], 0\n"
  "s_setprio 0\n"
  "v_exp_f32 v48, v48\n"
  "v_exp_f32 v49, v49\n"
  "v_exp_f32 v50, v50\n"
  "v_mfma_f32_32x32x8_bf16 v[120:135], v[140:141], v[154:155], v[120:135]\n"
  "v_exp_f32 v51, v51\n"
  "v_exp_f32 v52, v52\n"
  "v_exp_f32 v53, v53\n"
  "v_exp_f32 v54, v54\n"
  "s_waitcnt lgkmcnt(0)\n"
  "v_smfmac_f32_32x32x32_bf16 v[16:31], v[92:95], v[32:39], v157\n"
  "v_exp_f32 v55, v55\n"
  "v_exp_f32 v56, v56\n"
  "v_exp_f32 v57, v57\n"
  "v_smfmac_f32_32x32x32_bf16 v[16:31], v[96:99], v[40:47], v157\n"
  "v_exp_f32 v58, v58\n"
  "v_exp_f32 v59, v59\n"
  "v_exp_f32 v60, v60\n"
  "v_exp_f32 v61, v61\n"
  "v_exp_f32 v62, v62\n"
  "v_exp_f32 v63, v63\n"
  "s_barrier\n"
  "v_mfma_f32_32x32x16_bf16 v[32:47], v[100:103], v[144:147], 0\n"
  "ds_read_b128 v[136:139], v158 offset:24704\n"
  "s_setprio 3\n"
  "v_exp_f32 v120, v120\n"
  "v_exp_f32 v121, v121\n"
  "v_exp_f32 v122, v122\n"
  "v_mfma_f32_32x32x8_bf16 v[32:47], v[140:141], v[148:149], v[32:47]\n"
  "ds_read_b64 v[142:143], v159 offset:24704\n"
  "v_exp_f32 v123, v123\n"
  "v_exp_f32 v124, v124\n"
  "v_exp_f32 v125, v125\n"
  "v_exp_f32 v126, v126\n"
  "v_smfmac_f32_32x32x32_bf16 v[0:15], v[92:95], v[48:55], v157\n"
  "ds_read_b64_tr_b16 v[84:85], v64 offset:24704\n"
  "ds_read_b64_tr_b16 v[86:87], v65 offset:24704\n"
  "v_exp_f32 v127, v127\n"
  "v_exp_f32 v128, v128\n"
  "v_exp_f32 v129, v129\n"
  "v_smfmac_f32_32x32x32_bf16 v[0:15], v[96:99], v[56:63], v157\n"
  "ds_read_b64_tr_b16 v[88:89], v66 offset:24704\n"
  "ds_read_b64_tr_b16 v[90:91], v67 offset:24704\n"
  "v_exp_f32 v130, v130\n"
  "v_exp_f32 v131, v131\n"
  "v_exp_f32 v132, v132\n"
  "v_exp_f32 v133, v133\n"
  "v_exp_f32 v134, v134\n"
  "v_exp_f32 v135, v135\n"
  "s_waitcnt lgkmcnt(4)\n"
  "v_mfma_f32_32x32x16_bf16 v[48:63], v[136:139], v[150:153], 0\n"
  "s_setprio 2\n"
  "v_exp_f32 v32, v32\n"
  "v_exp_f32 v33, v33\n"
  "v_exp_f32 v34, v34\n"
  "v_mfma_f32_32x32x8_bf16 v[48:63], v[142:143], v[154:155], v[48:63]\n"
  "v_exp_f32 v35, v35\n"
  "v_exp_f32 v36, v36\n"
  "v_exp_f32 v37, v37\n"
  "v_exp_f32 v38, v38\n"
  "s_waitcnt lgkmcnt(0)\n"
  "v_smfmac_f32_32x32x32_bf16 v[16:31], v[84:87], v[120:127], v157\n"
  "v_exp_f32 v39, v39\n"
  "v_exp_f32 v40, v40\n"
  "v_exp_f32 v41, v41\n"
  "v_smfmac_f32_32x32x32_bf16 v[16:31], v[88:91], v[128:135], v157\n"
  "v_exp_f32 v42, v42\n"
  "v_exp_f32 v43, v43\n"
  "v_exp_f32 v44, v44\n"
  "v_exp_f32 v45, v45\n"
  "v_exp_f32 v46, v46\n"
  "v_exp_f32 v47, v47\n"
  "v_mfma_f32_32x32x16_bf16 v[120:135], v[136:139], v[144:147], 0\n"
  "ds_read_b128 v[100:103], v158 offset:25728\n"
  "s_setprio 1\n"
  "v_exp_f32 v48, v48\n"
  "v_exp_f32 v49, v49\n"
  "v_exp_f32 v50, v50\n"
  "v_mfma_f32_32x32x8_bf16 v[120:135], v[142:143], v[148:149], v[120:135]\n"
  "ds_read_b64 v[140:141], v160 offset:24704\n"
  "v_exp_f32 v51, v51\n"
  "v_exp_f32 v52, v52\n"
  "v_exp_f32 v53, v53\n"
  "v_exp_f32 v54, v54\n"
  "v_smfmac_f32_32x32x32_bf16 v[0:15], v[84:87], v[32:39], v157\n"
  "ds_read_b64_tr_b16 v[92:93], v68 offset:24704\n"
  "ds_read_b64_tr_b16 v[94:95], v69 offset:24704\n"
  "v_exp_f32 v55, v55\n"
  "v_exp_f32 v56, v56\n"
  "v_exp_f32 v57, v57\n"
  "v_smfmac_f32_32x32x32_bf16 v[0:15], v[88:91], v[40:47], v157\n"
  "ds_read_b64_tr_b16 v[96:97], v70 offset:24704\n"
  "ds_read_b64_tr_b16 v[98:99], v71 offset:24704\n"
  "v_exp_f32 v58, v58\n"
  "v_exp_f32 v59, v59\n"
  "v_exp_f32 v60, v60\n"
  "v_exp_f32 v61, v61\n"
  "v_exp_f32 v62, v62\n"
  "v_exp_f32 v63, v63\n"
  "s_waitcnt lgkmcnt(4)\n"
  "v_mfma_f32_32x32x16_bf16 v[32:47], v[100:103], v[150:153], 0\n"
  "s_setprio 0\n"
  "v_exp_f32 v120, v120\n"
  "v_exp_f32 v121, v121\n"
  "v_exp_f32 v122, v122\n"
  "v_mfma_f32_32x32x8_bf16 v[32:47], v[140:141], v[154:155], v[32:47]\n"
  "v_exp_f32 v123, v123\n"
  "v_exp_f32 v124, v124\n"
  "v_exp_f32 v125, v125\n"
  "v_exp_f32 v126, v126\n"
  "s_waitcnt lgkmcnt(0)\n"
  "v_smfmac_f32_32x32x32_bf16 v[16:31], v[92:95], v[48:55], v157\n"
  "v_exp_f32 v127, v127\n"
  "v_exp_f32 v128, v128\n"
  "v_exp_f32 v129, v129\n"
  "v_smfmac_f32_32x32x32_bf16 v[16:31], v[96:99], v[56:63], v157\n"
  "v_exp_f32 v130, v130\n"
  "v_exp_f32 v131, v131\n"
  "v_exp_f32 v132, v132\n"
  "v_exp_f32 v133, v133\n"
  "v_exp_f32 v134, v134\n"
  "v_exp_f32 v135, v135\n"
  "s_barrier\n"
  "v_mfma_f32_32x32x16_bf16 v[48:63], v[100:103], v[144:147], 0\n"
  "ds_read_b128 v[136:139], v158 offset:0\n"
  "s_setprio 3\n"
  "v_exp_f32 v32, v32\n"
  "v_exp_f32 v33, v33\n"
  "v_exp_f32 v34, v34\n"
  "v_mfma_f32_32x32x8_bf16 v[48:63], v[140:141], v[148:149], v[48:63]\n"
  "ds_read_b64 v[142:143], v159 offset:0\n"
  "v_exp_f32 v35, v35\n"
  "v_exp_f32 v36, v36\n"
  "v_exp_f32 v37, v37\n"
  "v_exp_f32 v38, v38\n"
  "v_smfmac_f32_32x32x32_bf16 v[0:15], v[92:95], v[120:127], v157\n"
  "ds_read_b64_tr_b16 v[84:85], v64 offset:0\n"
  "ds_read_b64_tr_b16 v[86:87], v65 offset:0\n"
  "v_exp_f32 v39, v39\n"
  "v_exp_f32 v40, v40\n"
  "v_exp_f32 v41, v41\n"
  "v_smfmac_f32_32x32x32_bf16 v[0:15], v[96:99], v[128:135], v157\n"
  "ds_read_b64_tr_b16 v[88:89], v66 offset:0\n"
  "ds_read_b64_tr_b16 v[90:91], v67 offset:0\n"
  "v_exp_f32 v42, v42\n"
  "v_exp_f32 v43, v43\n"
  "v_exp_f32 v44, v44\n"
  "v_exp_f32 v45, v45\n"
  "v_exp_f32 v46, v46\n"
  "v_exp_f32 v47, v47\n"
  "s_waitcnt lgkmcnt(4)\n"
  "v_mfma_f32_32x32x16_bf16 v[120:135], v[136:139], v[150:153], 0\n"
  "s_setprio 2\n"
  "v_exp_f32 v48, v48\n"
  "v_exp_f32 v49, v49\n"
  "v_exp_f32 v50, v50\n"
  "v_mfma_f32_32x32x8_bf16 v[120:135], v[142:143], v[154:155], v[120:135]\n"
  "v_exp_f32 v51, v51\n"
  "v_exp_f32 v52, v52\n"
  "v_exp_f32 v53, v53\n"
  "v_exp_f32 v54, v54\n"
  "s_waitcnt lgkmcnt(0)\n"
  "v_smfmac_f32_32x32x32_bf16 v[16:31], v[84:87], v[32:39], v157\n"
  "v_exp_f32 v55, v55\n"
  "v_exp_f32 v56, v56\n"
  "v_exp_f32 v57, v57\n"
  "v_smfmac_f32_32x32x32_bf16 v[16:31], v[88:91], v[40:47], v157\n"
  "v_exp_f32 v58, v58\n"
  "v_exp_f32 v59, v59\n"
  "v_exp_f32 v60, v60\n"
  "v_exp_f32 v61, v61\n"
  "v_exp_f32 v62, v62\n"
  "v_exp_f32 v63, v63\n"
  "v_mfma_f32_32x32x16_bf16 v[32:47], v[136:139], v[144:147], 0\n"
  "ds_read_b128 v[100:103], v158 offset:1024\n"
  "s_setprio 1\n"
  "v_exp_f32 v120, v120\n"
  "v_exp_f32 v121, v121\n"
  "v_exp_f32 v122, v122\n"
  "v_mfma_f32_32x32x8_bf16 v[32:47], v[142:143], v[148:149], v[32:47]\n"
  "ds_read_b64 v[140:141], v160 offset:0\n"
  "v_exp_f32 v123, v123\n"
  "v_exp_f32 v124, v124\n"
  "v_exp_f32 v125, v125\n"
  "v_exp_f32 v126, v126\n"
  "v_smfmac_f32_32x32x32_bf16 v[0:15], v[84:87], v[48:55], v157\n"
  "ds_read_b64_tr_b16 v[92:93], v68 offset:0\n"
  "ds_read_b64_tr_b16 v[94:95], v69 offset:0\n"
  "v_exp_f32 v127, v127\n"
  "v_exp_f32 v128, v128\n"
  "v_exp_f32 v129, v129\n"
  "v_smfmac_f32_32x32x32_bf16 v[0:15], v[88:91], v[56:63], v157\n"
  "ds_read_b64_tr_b16 v[96:97], v70 offset:0\n"
  "ds_read_b64_tr_b16 v[98:99], v71 offset:0\n"
  "v_exp_f32 v130, v130\n"
  "v_exp_f32 v131, v131\n"
  "v_exp_f32 v132, v132\n"
  "v_exp_f32 v133, v133\n"
  "v_exp_f32 v134, v134\n"
  "v_exp_f32 v135, v135\n"
  "s_waitcnt lgkmcnt(4)\n"
  "v_mfma_f32_32x32x16_bf16 v[48:63], v[100:103], v[150:153], 0\n"
  "s_setprio 0\n"
  "v_exp_f32 v32, v32\n"
  "v_exp_f32 v33, v33\n"
  "v_exp_f32 v34, v34\n"
  "v_mfma_f32_32x32x8_bf16 v[48:63], v[140:141], v[154:155], v[48:63]\n"
  "v_exp_f32 v35, v35\n"
  "v_exp_f32 v36, v36\n"
  "v_exp_f32 v37, v37\n"
  "v_exp_f32 v38, v38\n"
  "s_waitcnt lgkmcnt(0)\n"
  "v_smfmac_f32_32x32x32_bf16 v[16:31], v[92:95], v[120:127], v157\n"
  "v_exp_f32 v39, v39\n"
  "v_exp_f32 v40, v40\n"
  "v_exp_f32 v41, v41\n"
  "v_smfmac_f32_32x32x32_bf16 v[16:31], v[96:99], v[128:135], v157\n"
  "v_exp_f32 v42, v42\n"
  "v_exp_f32 v43, v43\n"
  "v_exp_f32 v44, v44\n"
  "v_exp_f32 v45, v45\n"
  "v_exp_f32 v46, v46\n"
  "v_exp_f32 v47, v47\n"
  "s_barrier\n"
  "v_mfma_f32_32x32x16_bf16 v[120:135], v[100:103], v[144:147], 0\n"
  "ds_read_b128 v[136:139], v158 offset:12352\n"
  "s_setprio 3\n"
  "v_exp_f32 v48, v48\n"
  "v_exp_f32 v49, v49\n"
  "v_exp_f32 v50, v50\n"
  "v_mfma_f32_32x32x8_bf16 v[120:135], v[140:141], v[148:149], v[120:135]\n"
  "ds_read_b64 v[142:143], v159 offset:12352\n"
  "v_exp_f32 v51, v51\n"
  "v_exp_f32 v52, v52\n"
  "v_exp_f32 v53, v53\n"
  "v_exp_f32 v54, v54\n"
  "v_smfmac_f32_32x32x32_bf16 v[0:15], v[92:95], v[32:39], v157\n"
  "ds_read_b64_tr_b16 v[84:85], v64 offset:12352\n"
  "ds_read_b64_tr_b16 v[86:87], v65 offset:12352\n"
  "v_exp_f32 v55, v55\n"
  "v_exp_f32 v56, v56\n"
  "v_exp_f32 v57, v57\n"
  "v_smfmac_f32_32x32x32_bf16 v[0:15], v[96:99], v[40:47], v157\n"
  "ds_read_b64_tr_b16 v[88:89], v66 offset:12352\n"
  "ds_read_b64_tr_b16 v[90:91], v67 offset:12352\n"
  "v_exp_f32 v58, v58\n"
  "v_exp_f32 v59, v59\n"
  "v_exp_f32 v60, v60\n"
  "v_exp_f32 v61, v61\n"
  "v_exp_f32 v62, v62\n"
  "v_exp_f32 v63, v63\n"
  "s_waitcnt lgkmcnt(4)\n"
  "v_mfma_f32_32x32x16_bf16 v[32:47], v[136:139], v[150:153], 0\n"
  "s_setprio 2\n"
  "v_exp_f32 v120, v120\n"
  "v_exp_f32 v121, v121\n"
  "v_exp_f32 v122, v122\n"
  "v_mfma_f32_32x32x8_bf16 v[32:47], v[142:143], v[154:155], v[32:47]\n"
  "v_exp_f32 v123, v123\n"
  "v_exp_f32 v124, v124\n"
  "v_exp_f32 v125, v125\n"
  "v_exp_f32 v126, v126\n"
  "s_waitcnt lgkmcnt(0)\n"
  "v_smfmac_f32_32x32x32_bf16 v[16:31], v[84:87], v[48:55], v157\n"
  "v_exp_f32 v127, v127\n"
  "v_exp_f32 v128, v128\n"
  "v_exp_f32 v129, v129\n"
  "v_smfmac_f32_32x32x32_bf16 v[16:31], v[88:91], v[56:63], v157\n"
  "v_exp_f32 v130, v130\n"
  "v_exp_f32 v131, v131\n"
  "v_exp_f32 v132, v132\n"
  "v_exp_f32 v133, v133\n"
  "v_exp_f32 v134, v134\n"
  "v_exp_f32 v135, v135\n"
  "v_mfma_f32_32x32x16_bf16 v[48:63], v[136:139], v[144:147], 0\n"
  "ds_read_b128 v[100:103], v158 offset:13376\n"
  "s_setprio 1\n"
  "v_exp_f32 v32, v32\n"
  "v_exp_f32 v33, v33\n"
  "v_exp_f32 v34, v34\n"
  "v_mfma_f32_32x32x8_bf16 v[48:63], v[142:143], v[148:149], v[48:63]\n"
  "ds_read_b64 v[140:141], v160 offset:12352\n"
  "v_exp_f32 v35, v35\n"
  "v_exp_f32 v36, v36\n"
  "v_exp_f32 v37, v37\n"
  "v_exp_f32 v38, v38\n"
  "v_smfmac_f32_32x32x32_bf16 v[0:15], v[84:87], v[120:127], v157\n"
  "ds_read_b64_tr_b16 v[92:93], v68 offset:12352\n"
  "ds_read_b64_tr_b16 v[94:95], v69 offset:12352\n"
  "v_exp_f32 v39, v39\n"
  "v_exp_f32 v40, v40\n"
  "v_exp_f32 v41, v41\n"
  "v_smfmac_f32_32x32x32_bf16 v[0:15], v[88:91], v[128:135], v157\n"
  "ds_read_b64_tr_b16 v[96:97], v70 offset:12352\n"
  "ds_read_b64_tr_b16 v[98:99], v71 offset:12352\n"
  "v_exp_f32 v42, v42\n"
  "v_exp_f32 v43, v43\n"
  "v_exp_f32 v44, v44\n"
  "v_exp_f32 v45, v45\n"
  "v_exp_f32 v46, v46\n"
  "v_exp_f32 v47, v47\n"
  "s_waitcnt lgkmcnt(4)\n"
  "v_mfma_f32_32x32x16_bf16 v[120:135], v[100:103], v[150:153], 0\n"
  "s_setprio 0\n"
  "v_exp_f32 v48, v48\n"
  "v_exp_f32 v49, v49\n"
  "v_exp_f32 v50, v50\n"
  "v_mfma_f32_32x32x8_bf16 v[120:135], v[140:141], v[154:155], v[120:135]\n"
  "v_exp_f32 v51, v51\n"
  "v_exp_f32 v52, v52\n"
  "v_exp_f32 v53, v53\n"
  "v_exp_f32 v54, v54\n"
  "s_waitcnt lgkmcnt(0)\n"
  "v_smfmac_f32_32x32x32_bf16 v[16:31], v[92:95], v[32:39], v157\n"
  "v_exp_f32 v55, v55\n"
  "v_exp_f32 v56, v56\n"
  "v_exp_f32 v57, v57\n"
  "v_smfmac_f32_32x32x32_bf16 v[16:31], v[96:99], v[40:47], v157\n"
  "v_exp_f32 v58, v58\n"
  "v_exp_f32 v59, v59\n"
  "v_exp_f32 v60, v60\n"
  "v_exp_f32 v61, v61\n"
  "v_exp_f32 v62, v62\n"
  "v_exp_f32 v63, v63\n"
  "s_barrier\n"
  "v_mfma_f32_32x32x16_bf16 v[32:47], v[100:103], v[144:147], 0\n"
  "ds_read_b128 v[136:139], v158 offset:24704\n"
  "s_setprio 3\n"
  "v_exp_f32 v120, v120\n"
  "v_exp_f32 v121, v121\n"
  "v_exp_f32 v122, v122\n"
  "v_mfma_f32_32x32x8_bf16 v[32:47], v[140:141], v[148:149], v[32:47]\n"
  "ds_read_b64 v[142:143], v159 offset:24704\n"
  "v_exp_f32 v123, v123\n"
  "v_exp_f32 v124, v124\n"
  "v_exp_f32 v125, v125\n"
  "v_exp_f32 v126, v126\n"
  "v_smfmac_f32_32x32x32_bf16 v[0:15], v[92:95], v[48:55], v157\n"
  "ds_read_b64_tr_b16 v[84:85], v64 offset:24704\n"
  "ds_read_b64_tr_b16 v[86:87], v65 offset:24704\n"
  "v_exp_f32 v127, v127\n"
  "v_exp_f32 v128, v128\n"
  "v_exp_f32 v129, v129\n"
  "v_smfmac_f32_32x32x32_bf16 v[0:15], v[96:99], v[56:63], v157\n"
  "ds_read_b64_tr_b16 v[88:89], v66 offset:24704\n"
  "ds_read_b64_tr_b16 v[90:91], v67 offset:24704\n"
  "v_exp_f32 v130, v130\n"
  "v_exp_f32 v131, v131\n"
  "v_exp_f32 v132, v132\n"
  "v_exp_f32 v133, v133\n"
  "v_exp_f32 v134, v134\n"
  "v_exp_f32 v135, v135\n"
  "s_waitcnt lgkmcnt(4)\n"
  "v_mfma_f32_32x32x16_bf16 v[48:63], v[136:139], v[150:153], 0\n"
  "s_setprio 2\n"
  "v_exp_f32 v32, v32\n"
  "v_exp_f32 v33, v33\n"
  "v_exp_f32 v34, v34\n"
  "v_mfma_f32_32x32x8_bf16 v[48:63], v[142:143], v[154:155], v[48:63]\n"
  "v_exp_f32 v35, v35\n"
  "v_exp_f32 v36, v36\n"
  "v_exp_f32 v37, v37\n"
  "v_exp_f32 v38, v38\n"
  "s_waitcnt lgkmcnt(0)\n"
  "v_smfmac_f32_32x32x32_bf16 v[16:31], v[84:87], v[120:127], v157\n"
  "v_exp_f32 v39, v39\n"
  "v_exp_f32 v40, v40\n"
  "v_exp_f32 v41, v41\n"
  "v_smfmac_f32_32x32x32_bf16 v[16:31], v[88:91], v[128:135], v157\n"
  "v_exp_f32 v42, v42\n"
  "v_exp_f32 v43, v43\n"
  "v_exp_f32 v44, v44\n"
  "v_exp_f32 v45, v45\n"
  "v_exp_f32 v46, v46\n"
  "v_exp_f32 v47, v47\n"
  "v_mfma_f32_32x32x16_bf16 v[120:135], v[136:139], v[144:147], 0\n"
  "ds_read_b128 v[100:103], v158 offset:25728\n"
  "s_setprio 1\n"
  "v_exp_f32 v48, v48\n"
  "v_exp_f32 v49, v49\n"
  "v_exp_f32 v50, v50\n"
  "v_mfma_f32_32x32x8_bf16 v[120:135], v[142:143], v[148:149], v[120:135]\n"
  "ds_read_b64 v[140:141], v160 offset:24704\n"
  "v_exp_f32 v51, v51\n"
  "v_exp_f32 v52, v52\n"
  "v_exp_f32 v53, v53\n"
  "v_exp_f32 v54, v54\n"
  "v_smfmac_f32_32x32x32_bf16 v[0:15], v[84:87], v[32:39], v157\n"
  "ds_read_b64_tr_b16 v[92:93], v68 offset:24704\n"
  "ds_read_b64_tr_b16 v[94:95], v69 offset:24704\n"
  "v_exp_f32 v55, v55\n"
  "v_exp_f32 v56, v56\n"
  "v_exp_f32 v57, v57\n"
  "v_smfmac_f32_32x32x32_bf16 v[0:15], v[88:91], v[40:47], v157\n"
  "ds_read_b64_tr_b16 v[96:97], v70 offset:24704\n"
  "ds_read_b64_tr_b16 v[98:99], v71 offset:24704\n"
  "v_exp_f32 v58, v58\n"
  "v_exp_f32 v59, v59\n"
  "v_exp_f32 v60, v60\n"
  "v_exp_f32 v61, v61\n"
  "v_exp_f32 v62, v62\n"
  "v_exp_f32 v63, v63\n"
  "s_waitcnt lgkmcnt(4)\n"
  "v_mfma_f32_32x32x16_bf16 v[32:47], v[100:103], v[150:153], 0\n"
  "s_setprio 0\n"
  "v_exp_f32 v120, v120\n"
  "v_exp_f32 v121, v121\n"
  "v_exp_f32 v122, v122\n"
  "v_mfma_f32_32x32x8_bf16 v[32:47], v[140:141], v[154:155], v[32:47]\n"
  "v_exp_f32 v123, v123\n"
  "v_exp_f32 v124, v124\n"
  "v_exp_f32 v125, v125\n"
  "v_exp_f32 v126, v126\n"
  "s_waitcnt lgkmcnt(0)\n"
  "v_smfmac_f32_32x32x32_bf16 v[16:31], v[92:95], v[48:55], v157\n"
  "v_exp_f32 v127, v127\n"
  "v_exp_f32 v128, v128\n"
  "v_exp_f32 v129, v129\n"
  "v_smfmac_f32_32x32x32_bf16 v[16:31], v[96:99], v[56:63], v157\n"
  "v_exp_f32 v130, v130\n"
  "v_exp_f32 v131, v131\n"
  "v_exp_f32 v132, v132\n"
  "v_exp_f32 v133, v133\n"
  "v_exp_f32 v134, v134\n"
  "v_exp_f32 v135, v135\n"
  "s_barrier\n"
  "v_mfma_f32_32x32x16_bf16 v[48:63], v[100:103], v[144:147], 0\n"
  "ds_read_b128 v[136:139], v158 offset:0\n"
  "s_setprio 3\n"
  "v_exp_f32 v32, v32\n"
  "v_exp_f32 v33, v33\n"
  "v_exp_f32 v34, v34\n"
  "v_mfma_f32_32x32x8_bf16 v[48:63], v[140:141], v[148:149], v[48:63]\n"
  "ds_read_b64 v[142:143], v159 offset:0\n"
  "v_exp_f32 v35, v35\n"
  "v_exp_f32 v36, v36\n"
  "v_exp_f32 v37, v37\n"
  "v_exp_f32 v38, v38\n"
  "v_smfmac_f32_32x32x32_bf16 v[0:15], v[92:95], v[120:127], v157\n"
  "ds_read_b64_tr_b16 v[84:85], v64 offset:0\n"
  "ds_read_b64_tr_b16 v[86:87], v65 offset:0\n"
  "v_exp_f32 v39, v39\n"
  "v_exp_f32 v40, v40\n"
  "v_exp_f32 v41, v41\n"
  "v_smfmac_f32_32x32x32_bf16 v[0:15], v[96:99], v[128:135], v157\n"
  "ds_read_b64_tr_b16 v[88:89], v66 offset:0\n"
  "ds_read_b64_tr_b16 v[90:91], v67 offset:0\n"
  "v_exp_f32 v42, v42\n"
  "v_exp_f32 v43, v43\n"
  "v_exp_f32 v44, v44\n"
  "v_exp_f32 v45, v45\n"
  "v_exp_f32 v46, v46\n"
  "v_exp_f32 v47, v47\n"
  "s_waitcnt lgkmcnt(4)\n"
  "v_mfma_f32_32x32x16_bf16 v[120:135], v[136:139], v[150:153], 0\n"
  "s_setprio 2\n"
  "v_exp_f32 v48, v48\n"
  "v_exp_f32 v49, v49\n"
  "v_exp_f32 v50, v50\n"
  "v_mfma_f32_32x32x8_bf16 v[120:135], v[142:143], v[154:155], v[120:135]\n"
  "v_exp_f32 v51, v51\n"
  "v_exp_f32 v52, v52\n"
  "v_exp_f32 v53, v53\n"
  "v_exp_f32 v54, v54\n"
  "s_waitcnt lgkmcnt(0)\n"
  "v_smfmac_f32_32x32x32_bf16 v[16:31], v[84:87], v[32:39], v157\n"
  "v_exp_f32 v55, v55\n"
  "v_exp_f32 v56, v56\n"
  "v_exp_f32 v57, v57\n"
  "v_smfmac_f32_32x32x32_bf16 v[16:31], v[88:91], v[40:47], v157\n"
  "v_exp_f32 v58, v58\n"
  "v_exp_f32 v59, v59\n"
  "v_exp_f32 v60, v60\n"
  "v_exp_f32 v61, v61\n"
  "v_exp_f32 v62, v62\n"
  "v_exp_f32 v63, v63\n"
  "v_mfma_f32_32x32x16_bf16 v[32:47], v[136:139], v[144:147], 0\n"
  "ds_read_b128 v[100:103], v158 offset:1024\n"
  "s_setprio 1\n"
  "v_exp_f32 v120, v120\n"
  "v_exp_f32 v121, v121\n"
  "v_exp_f32 v122, v122\n"
  "v_mfma_f32_32x32x8_bf16 v[32:47], v[142:143], v[148:149], v[32:47]\n"
  "ds_read_b64 v[140:141], v160 offset:0\n"
  "v_exp_f32 v123, v123\n"
  "v_exp_f32 v124, v124\n"
  "v_exp_f32 v125, v125\n"
  "v_exp_f32 v126, v126\n"
  "v_smfmac_f32_32x32x32_bf16 v[0:15], v[84:87], v[48:55], v157\n"
  "ds_read_b64_tr_b16 v[92:93], v68 offset:0\n"
  "ds_read_b64_tr_b16 v[94:95], v69 offset:0\n"
  "v_exp_f32 v127, v127\n"
  "v_exp_f32 v128, v128\n"
  "v_exp_f32 v129, v129\n"
  "v_smfmac_f32_32x32x32_bf16 v[0:15], v[88:91], v[56:63], v157\n"
  "ds_read_b64_tr_b16 v[96:97], v70 offset:0\n"
  "ds_read_b64_tr_b16 v[98:99], v71 offset:0\n"
  "v_exp_f32 v130, v130\n"
  "v_exp_f32 v131, v131\n"
  "v_exp_f32 v132, v132\n"
  "v_exp_f32 v133, v133\n"
  "v_exp_f32 v134, v134\n"
  "v_exp_f32 v135, v135\n"
  "s_waitcnt lgkmcnt(4)\n"
  "v_mfma_f32_32x32x16_bf16 v[48:63], v[100:103], v[150:153], 0\n"
  "s_setprio 0\n"
  "v_exp_f32 v32, v32\n"
  "v_exp_f32 v33, v33\n"
  "v_exp_f32 v34, v34\n"
  "v_mfma_f32_32x32x8_bf16 v[48:63], v[140:141], v[154:155], v[48:63]\n"
  "v_exp_f32 v35, v35\n"
  "v_exp_f32 v36, v36\n"
  "v_exp_f32 v37, v37\n"
  "v_exp_f32 v38, v38\n"
  "s_waitcnt lgkmcnt(0)\n"
  "v_smfmac_f32_32x32x32_bf16 v[16:31], v[92:95], v[120:127], v157\n"
  "v_exp_f32 v39, v39\n"
  "v_exp_f32 v40, v40\n"
  "v_exp_f32 v41, v41\n"
  "v_smfmac_f32_32x32x32_bf16 v[16:31], v[96:99], v[128:135], v157\n"
  "v_exp_f32 v42, v42\n"
  "v_exp_f32 v43, v43\n"
  "v_exp_f32 v44, v44\n"
  "v_exp_f32 v45, v45\n"
  "v_exp_f32 v46, v46\n"
  "v_exp_f32 v47, v47\n"
  "s_barrier\n"
  "v_mfma_f32_32x32x16_bf16 v[120:135], v[100:103], v[144:147], 0\n"
  "ds_read_b128 v[136:139], v158 offset:12352\n"
  "s_setprio 3\n"
  "v_exp_f32 v48, v48\n"
  "v_exp_f32 v49, v49\n"
  "v_exp_f32 v50, v50\n"
  "v_mfma_f32_32x32x8_bf16 v[120:135], v[140:141], v[148:149], v[120:135]\n"
  "ds_read_b64 v[142:143], v159 offset:12352\n"
  "v_exp_f32 v51, v51\n"
  "v_exp_f32 v52, v52\n"
  "v_exp_f32 v53, v53\n"
  "v_exp_f32 v54, v54\n"
  "v_smfmac_f32_32x32x32_bf16 v[0:15], v[92:95], v[32:39], v157\n"
  "ds_read_b64_tr_b16 v[84:85], v64 offset:12352\n"
  "ds_read_b64_tr_b16 v[86:87], v65 offset:12352\n"
  "v_exp_f32 v55, v55\n"
  "v_exp_f32 v56, v56\n"
  "v_exp_f32 v57, v57\n"
  "v_smfmac_f32_32x32x32_bf16 v[0:15], v[96:99], v[40:47], v157\n"
  "ds_read_b64_tr_b16 v[88:89], v66 offset:12352\n"
  "ds_read_b64_tr_b16 v[90:91], v67 offset:12352\n"
  "v_exp_f32 v58, v58\n"
  "v_exp_f32 v59, v59\n"
  "v_exp_f32 v60, v60\n"
  "v_exp_f32 v61, v61\n"
  "v_exp_f32 v62, v62\n"
  "v_exp_f32 v63, v63\n"
  "s_waitcnt lgkmcnt(4)\n"
  "v_mfma_f32_32x32x16_bf16 v[32:47], v[136:139], v[150:153], 0\n"
  "s_setprio 2\n"
  "v_exp_f32 v120, v120\n"
  "v_exp_f32 v121, v121\n"
  "v_exp_f32 v122, v122\n"
  "v_mfma_f32_32x32x8_bf16 v[32:47], v[142:143], v[154:155], v[32:47]\n"
  "v_exp_f32 v123, v123\n"
  "v_exp_f32 v124, v124\n"
  "v_exp_f32 v125, v125\n"
  "v_exp_f32 v126, v126\n"
  "s_waitcnt lgkmcnt(0)\n"
  "v_smfmac_f32_32x32x32_bf16 v[16:31], v[84:87], v[48:55], v157\n"
  "v_exp_f32 v127, v127\n"
  "v_exp_f32 v128, v128\n"
  "v_exp_f32 v129, v129\n"
  "v_smfmac_f32_32x32x32_bf16 v[16:31], v[88:91], v[56:63], v157\n"
  "v_exp_f32 v130, v130\n"
  "v_exp_f32 v131, v131\n"
  "v_exp_f32 v132, v132\n"
  "v_exp_f32 v133, v133\n"
  "v_exp_f32 v134, v134\n"
  "v_exp_f32 v135, v135\n"
  "v_mfma_f32_32x32x16_bf16 v[48:63], v[136:139], v[144:147], 0\n"
  "ds_read_b128 v[100:103], v158 offset:13376\n"
  "s_setprio 1\n"
  "v_exp_f32 v32, v32\n"
  "v_exp_f32 v33, v33\n"
  "v_exp_f32 v34, v34\n"
  "v_mfma_f32_32x32x8_bf16 v[48:63], v[142:143], v[148:149], v[48:63]\n"
  "ds_read_b64 v[140:141], v160 offset:12352\n"
  "v_exp_f32 v35, v35\n"
  "v_exp_f32 v36, v36\n"
  "v_exp_f32 v37, v37\n"
  "v_exp_f32 v38, v38\n"
  "v_smfmac_f32_32x32x32_bf16 v[0:15], v[84:87], v[120:127], v157\n"
  "ds_read_b64_tr_b16 v[92:93], v68 offset:12352\n"
  "ds_read_b64_tr_b16 v[94:95], v69 offset:12352\n"
  "v_exp_f32 v39, v39\n"
  "v_exp_f32 v40, v40\n"
  "v_exp_f32 v41, v41\n"
  "v_smfmac_f32_32x32x32_bf16 v[0:15], v[88:91], v[128:135], v157\n"
  "ds_read_b64_tr_b16 v[96:97], v70 offset:12352\n"
  "ds_read_b64_tr_b16 v[98:99], v71 offset:12352\n"
  "v_exp_f32 v42, v42\n"
  "v_exp_f32 v43, v43\n"
  "v_exp_f32 v44, v44\n"
  "v_exp_f32 v45, v45\n"
  "v_exp_f32 v46, v46\n"
  "v_exp_f32 v47, v47\n"
  "s_waitcnt lgkmcnt(4)\n"
  "v_mfma_f32_32x32x16_bf16 v[120:135], v[100:103], v[150:153], 0\n"
  "s_setprio 0\n"
  "v_exp_f32 v48, v48\n"
  "v_exp_f32 v49, v49\n"
  "v_exp_f32 v50, v50\n"
  "v_mfma_f32_32x32x8_bf16 v[120:135], v[140:141], v[154:155], v[120:135]\n"
  "v_exp_f32 v51, v51\n"
  "v_exp_f32 v52, v52\n"
  "v_exp_f32 v53, v53\n"
  "v_exp_f32 v54, v54\n"
  "s_waitcnt lgkmcnt(0)\n"
  "v_smfmac_f32_32x32x32_bf16 v[16:31], v[92:95], v[32:39], v157\n"
  "v_exp_f32 v55, v55\n"
  "v_exp_f32 v56, v56\n"
  "v_exp_f32 v57, v57\n"
  "v_smfmac_f32_32x32x32_bf16 v[16:31], v[96:99], v[40:47], v157\n"
  "v_exp_f32 v58, v58\n"
  "v_exp_f32 v59, v59\n"
  "v_exp_f32 v60, v60\n"
  "v_exp_f32 v61, v61\n"
  "v_exp_f32 v62, v62\n"
  "v_exp_f32 v63, v63\n"
  "s_barrier\n"
  "v_mfma_f32_32x32x16_bf16 v[32:47], v[100:103], v[144:147], 0\n"
  "ds_read_b128 v[136:139], v158 offset:24704\n"
  "s_setprio 3\n"
  "v_exp_f32 v120, v120\n"
  "v_exp_f32 v121, v121\n"
  "v_exp_f32 v122, v122\n"
  "v_mfma_f32_32x32x8_bf16 v[32:47], v[140:141], v[148:149], v[32:47]\n"
  "ds_read_b64 v[142:143], v159 offset:24704\n"
  "v_exp_f32 v123, v123\n"
  "v_exp_f32 v124, v124\n"
  "v_exp_f32 v125, v125\n"
  "v_exp_f32 v126, v126\n"
  "v_smfmac_f32_32x32x32_bf16 v[0:15], v[92:95], v[48:55], v157\n"
  "ds_read_b64_tr_b16 v[84:85], v64 offset:24704\n"
  "ds_read_b64_tr_b16 v[86:87], v65 offset:24704\n"
  "v_exp_f32 v127, v127\n"
  "v_exp_f32 v128, v128\n"
  "v_exp_f32 v129, v129\n"
  "v_smfmac_f32_32x32x32_bf16 v[0:15], v[96:99], v[56:63], v157\n"
  "ds_read_b64_tr_b16 v[88:89], v66 offset:24704\n"
  "ds_read_b64_tr_b16 v[90:91], v67 offset:24704\n"
  "v_exp_f32 v130, v130\n"
  "v_exp_f32 v131, v131\n"
  "v_exp_f32 v132, v132\n"
  "v_exp_f32 v133, v133\n"
  "v_exp_f32 v134, v134\n"
  "v_exp_f32 v135, v135\n"
  "s_waitcnt lgkmcnt(4)\n"
  "v_mfma_f32_32x32x16_bf16 v[48:63], v[136:139], v[150:153], 0\n"
  "s_setprio 2\n"
  "v_exp_f32 v32, v32\n"
  "v_exp_f32 v33, v33\n"
  "v_exp_f32 v34, v34\n"
  "v_mfma_f32_32x32x8_bf16 v[48:63], v[142:143], v[154:155], v[48:63]\n"
  "v_exp_f32 v35, v35\n"
  "v_exp_f32 v36, v36\n"
  "v_exp_f32 v37, v37\n"
  "v_exp_f32 v38, v38\n"
  "s_waitcnt lgkmcnt(0)\n"
  "v_smfmac_f32_32x32x32_bf16 v[16:31], v[84:87], v[120:127], v157\n"
  "v_exp_f32 v39, v39\n"
  "v_exp_f32 v40, v40\n"
  "v_exp_f32 v41, v41\n"
  "v_smfmac_f32_32x32x32_bf16 v[16:31], v[88:91], v[128:135], v157\n"
  "v_exp_f32 v42, v42\n"
  "v_exp_f32 v43, v43\n"
  "v_exp_f32 v44, v44\n"
  "v_exp_f32 v45, v45\n"
  "v_exp_f32 v46, v46\n"
  "v_exp_f32 v47, v47\n"
  "v_mfma_f32_32x32x16_bf16 v[120:135], v[136:139], v[144:147], 0\n"
  "ds_read_b128 v[100:103], v158 offset:25728\n"
  "s_setprio 1\n"
  "v_exp_f32 v48, v48\n"
  "v_exp_f32 v49, v49\n"
  "v_exp_f32 v50, v50\n"
  "v_mfma_f32_32x32x8_bf16 v[120:135], v[142:143], v[148:149], v[120:135]\n"
  "ds_read_b64 v[140:141], v160 offset:24704\n"
  "v_exp_f32 v51, v51\n"
  "v_exp_f32 v52, v52\n"
  "v_exp_f32 v53, v53\n"
  "v_exp_f32 v54, v54\n"
  "v_smfmac_f32_32x32x32_bf16 v[0:15], v[84:87], v[32:39], v157\n"
  "ds_read_b64_tr_b16 v[92:93], v68 offset:24704\n"
  "ds_read_b64_tr_b16 v[94:95], v69 offset:24704\n"
  "v_exp_f32 v55, v55\n"
  "v_exp_f32 v56, v56\n"
  "v_exp_f32 v57, v57\n"
  "v_smfmac_f32_32x32x32_bf16 v[0:15], v[88:91], v[40:47], v157\n"
  "ds_read_b64_tr_b16 v[96:97], v70 offset:24704\n"
  "ds_read_b64_tr_b16 v[98:99], v71 offset:24704\n"
  "v_exp_f32 v58, v58\n"
  "v_exp_f32 v59, v59\n"
  "v_exp_f32 v60, v60\n"
  "v_exp_f32 v61, v61\n"
  "v_exp_f32 v62, v62\n"
  "v_exp_f32 v63, v63\n"
  "s_waitcnt lgkmcnt(4)\n"
  "v_mfma_f32_32x32x16_bf16 v[32:47], v[100:103], v[150:153], 0\n"
  "s_setprio 0\n"
  "v_exp_f32 v120, v120\n"
  "v_exp_f32 v121, v121\n"
  "v_exp_f32 v122, v122\n"
  "v_mfma_f32_32x32x8_bf16 v[32:47], v[140:141], v[154:155], v[32:47]\n"
  "v_exp_f32 v123, v123\n"
  "v_exp_f32 v124, v124\n"
  "v_exp_f32 v125, v125\n"
  "v_exp_f32 v126, v126\n"
  "s_waitcnt lgkmcnt(0)\n"
  "v_smfmac_f32_32x32x32_bf16 v[16:31], v[92:95], v[48:55], v157\n"
  "v_exp_f32 v127, v127\n"
  "v_exp_f32 v128, v128\n"
  "v_exp_f32 v129, v129\n"
  "v_smfmac_f32_32x32x32_bf16 v[16:31], v[96:99], v[56:63], v157\n"
  "v_exp_f32 v130, v130\n"
  "v_exp_f32 v131, v131\n"
  "v_exp_f32 v132, v132\n"
  "v_exp_f32 v133, v133\n"
  "v_exp_f32 v134, v134\n"
  "v_exp_f32 v135, v135\n"
  "s_barrier\n"
  "v_mfma_f32_32x32x16_bf16 v[48:63], v[100:103], v[144:147], 0\n"
  "ds_read_b128 v[136:139], v158 offset:0\n"
  "s_setprio 3\n"
  "v_exp_f32 v32, v32\n"
  "v_exp_f32 v33, v33\n"
  "v_exp_f32 v34, v34\n"
  "v_mfma_f32_32x32x8_bf16 v[48:63], v[140:141], v[148:149], v[48:63]\n"
  "ds_read_b64 v[142:143], v159 offset:0\n"
  "v_exp_f32 v35, v35\n"
  "v_exp_f32 v36, v36\n"
  "v_exp_f32 v37, v37\n"
  "v_exp_f32 v38, v38\n"
  "v_smfmac_f32_32x32x32_bf16 v[0:15], v[92:95], v[120:127], v157\n"
  "ds_read_b64_tr_b16 v[84:85], v64 offset:0\n"
  "ds_read_b64_tr_b16 v[86:87], v65 offset:0\n"
  "v_exp_f32 v39, v39\n"
  "v_exp_f32 v40, v40\n"
  "v_exp_f32 v41, v41\n"
  "v_smfmac_f32_32x32x32_bf16 v[0:15], v[96:99], v[128:135], v157\n"
  "ds_read_b64_tr_b16 v[88:89], v66 offset:0\n"
  "ds_read_b64_tr_b16 v[90:91], v67 offset:0\n"
  "v_exp_f32 v42, v42\n"
  "v_exp_f32 v43, v43\n"
  "v_exp_f32 v44, v44\n"
  "v_exp_f32 v45, v45\n"
  "v_exp_f32 v46, v46\n"
  "v_exp_f32 v47, v47\n"
  "s_waitcnt lgkmcnt(4)\n"
  "v_mfma_f32_32x32x16_bf16 v[120:135], v[136:139], v[150:153], 0\n"
  "s_setprio 2\n"
  "v_exp_f32 v48, v48\n"
  "v_exp_f32 v49, v49\n"
  "v_exp_f32 v50, v50\n"
  "v_mfma_f32_32x32x8_bf16 v[120:135], v[142:143], v[154:155], v[120:135]\n"
  "v_exp_f32 v51, v51\n"
  "v_exp_f32 v52, v52\n"
  "v_exp_f32 v53, v53\n"
  "v_exp_f32 v54, v54\n"
  "s_waitcnt lgkmcnt(0)\n"
  "v_smfmac_f32_32x32x32_bf16 v[16:31], v[84:87], v[32:39], v157\n"
  "v_exp_f32 v55, v55\n"
  "v_exp_f32 v56, v56\n"
  "v_exp_f32 v57, v57\n"
  "v_smfmac_f32_32x32x32_bf16 v[16:31], v[88:91], v[40:47], v157\n"
  "v_exp_f32 v58, v58\n"
  "v_exp_f32 v59, v59\n"
  "v_exp_f32 v60, v60\n"
  "v_exp_f32 v61, v61\n"
  "v_exp_f32 v62, v62\n"
  "v_exp_f32 v63, v63\n"
  "v_mfma_f32_32x32x16_bf16 v[32:47], v[136:139], v[144:147], 0\n"
  "s_setprio 1\n"
  "v_exp_f32 v120, v120\n"
  "v_exp_f32 v121, v121\n"
  "v_exp_f32 v122, v122\n"
  "v_mfma_f32_32x32x8_bf16 v[32:47], v[142:143], v[148:149], v[32:47]\n"
  "v_exp_f32 v123, v123\n"
  "v_exp_f32 v124, v124\n"
  "v_exp_f32 v125, v125\n"
  "v_exp_f32 v126, v126\n"
  "v_smfmac_f32_32x32x32_bf16 v[0:15], v[84:87], v[48:55], v157\n"
  "ds_read_b64_tr_b16 v[92:93], v68 offset:0\n"
  "ds_read_b64_tr_b16 v[94:95], v69 offset:0\n"
  "v_exp_f32 v127, v127\n"
  "v_exp_f32 v128, v128\n"
  "v_exp_f32 v129, v129\n"
  "v_smfmac_f32_32x32x32_bf16 v[0:15], v[88:91], v[56:63], v157\n"
  "ds_read_b64_tr_b16 v[96:97], v70 offset:0\n"
  "ds_read_b64_tr_b16 v[98:99], v71 offset:0\n"
  "v_exp_f32 v130, v130\n"
  "v_exp_f32 v131, v131\n"
  "v_exp_f32 v132, v132\n"
  "v_exp_f32 v133, v133\n"
  "v_exp_f32 v134, v134\n"
  "v_exp_f32 v135, v135\n"
  "s_waitcnt lgkmcnt(4)\n"
  "s_setprio 0\n"
  "v_exp_f32 v32, v32\n"
  "v_exp_f32 v33, v33\n"
  "v_exp_f32 v34, v34\n"
  "v_exp_f32 v35, v35\n"
  "v_exp_f32 v36, v36\n"
  "v_exp_f32 v37, v37\n"
  "v_exp_f32 v38, v38\n"
  "s_waitcnt lgkmcnt(0)\n"
  "v_smfmac_f32_32x32x32_bf16 v[16:31], v[92:95], v[120:127], v157\n"
  "v_exp_f32 v39, v39\n"
  "v_exp_f32 v40, v40\n"
  "v_exp_f32 v41, v41\n"
  "v_smfmac_f32_32x32x32_bf16 v[16:31], v[96:99], v[128:135], v157\n"
  "v_exp_f32 v42, v42\n"
  "v_exp_f32 v43, v43\n"
  "v_exp_f32 v44, v44\n"
  "v_exp_f32 v45, v45\n"
  "v_exp_f32 v46, v46\n"
  "v_exp_f32 v47, v47\n"
  "v_smfmac_f32_32x32x32_bf16 v[0:15], v[92:95], v[32:39], v157\n"
  "s_nop 0\n"
  "v_smfmac_f32_32x32x32_bf16 v[0:15], v[96:99], v[40:47], v157\n"
  "s_nop 3\n"
  "ds_write_b128 v161, v[16:19] offset:0\n"
  "ds_write_b128 v161, v[20:23] offset:1024\n"
  "ds_write_b128 v161, v[24:27] offset:2048\n"
  "ds_write_b32 v163, v28\n"
  "v_xor_b32 v163, 0x400, v163\n"
  "s_waitcnt lgkmcnt(0)\n"
  "s_barrier\n"
  "ds_read_b32 v60, v163\n"
  "ds_read_b128 v[48:51], v162 offset:0\n"
  "ds_read_b128 v[52:55], v162 offset:1024\n"
  "ds_read_b128 v[56:59], v162 offset:2048\n"
  "s_waitcnt lgkmcnt(3)\n"
  "v_add_f32 v12, v12, v60\n"
  "s_waitcnt lgkmcnt(0)\n"
  "v_pk_add_f32 v[0:1], v[0:1], v[48:49]\n"
  "v_pk_add_f32 v[2:3], v[2:3], v[50:51]\n"
  "v_pk_add_f32 v[4:5], v[4:5], v[52:53]\n"
  "v_pk_add_f32 v[6:7], v[6:7], v[54:55]\n"
  "v_pk_add_f32 v[8:9], v[8:9], v[56:57]\n"
  "v_pk_add_f32 v[10:11], v[10:11], v[58:59]\n"
  "v_maximum3_f32 v48, |v0|, |v1|, |v2|\n"
  "v_maximum3_f32 v49, |v3|, |v4|, |v5|\n"
  "v_maximum3_f32 v50, |v6|, |v7|, |v8|\n"
  "v_maximum3_f32 v51, |v9|, |v10|, |v11|\n"
  "v_cmp_nlt_f32 vcc, 0x39800000, v12\n"
  "s_mov_b32 s42, 0x71800000\n"
  "v_maximum3_f32 v48, v48, v49, v50\n"
  "v_cmp_ngt_f32 s[40:41], s42, v12\n"
  "s_mov_b32 s42, 0x7f800000\n"
  "v_maximum3_f32 v48, v48, v51, v51\n"
  "s_or_b64 vcc, vcc, s[40:41]\n"
  "v_cmp_ngt_f32 s[40:41], s42, v48\n"
  "s_or_b64 s[44:45], vcc, s[40:41]\n"
  "s_branch 60f\n"
  "310:\n"
  "s_add_u32 s44, s44, 1\n"
  "s_cmp_gt_u32 s44, 524288\n"
  "s_cbranch_scc1 410b\n"
  "buffer_load_dwordx4 v[12:15], v150, s[16:19], s64 offen offset:0 sc1\n"
  "s_waitcnt vmcnt(0)\n"
  "s_branch 210b\n"
  "60:\n"
        : "={v[0:15]}"(o), "={v[144:147]}"(qm), "={v[148:149]}"(qr), "={v156}"(l16), "={v167}"(dbg), "={s3}"(wid), "+{s[6:7]}"(tab), "={s[8:11]}"(rx), "={s[16:19]}"(rw), "={s[32:35]}"(ro),
          "={s[44:45]}"(bad), "={s[46:47]}"(tag), "={s[84:99]}"(t2), "+{s[0:1]}"(ka)
        : "{v0}"(threadIdx.x), "{s2}"(blockIdx.x), "{s3}"((unsigned)(size_t)(__attribute__((address_space(3))) char*)smem)
        : "v16", "v17", "v18", "v19", "v20", "v21", "v22", "v23", "v24", "v25", "v26", "v27", "v28", "v29", "v30", "v31", "v32", "v33", "v34", "v35", "v36", "v37", "v38", "v39", "v40", "v41", "v42", "v43", "v44", "v45", "v46", "v47", "v48", "v49", "v50", "v51", "v52", "v53", "v54", "v55", "v56", "v57", "v58", "v59", "v60", "v61", "v62", "v63", "v64", "v65", "v66", "v67", "v68", "v69", "v70", "v71", "v72", "v73", "v74", "v75", "v76", "v77", "v78", "v79", "v80", "v81", "v82", "v83", "v84", "v85", "v86", "v87", "v88", "v89", "v90", "v91", "v92", "v93", "v94", "v95", "v96", "v97", "v98", "v99", "v100", "v101", "v102", "v103", "v104", "v105", "v106", "v107", "v108", "v109", "v110", "v111", "v112", "v113", "v114", "v115", "v116", "v117", "v118", "v119", "v120", "v121", "v122", "v123", "v124", "v125", "v126", "v127", "v128", "v129", "v130", "v131", "v132", "v133", "v134", "v135", "v136", "v137", "v138", "v139", "v140", "v141", "v142", "v143", "v150", "v151", "v152", "v153", "v154", "v155", "v157", "v158", "v159", "v160", "v161", "v162", "v163", "v164", "v165", "v166", "s4", "s5", "s12", "s13", "s14", "s15", "s20", "s21", "s22", "s23", "s24", "s25", "s26", "s27", "s28", "s29", "s30", "s31", "s36", "s37", "s38", "s39", "s40", "s41", "s42", "s43", "s48", "s49", "s50", "s51", "s52", "s53", "s54", "s55", "s56", "s57", "s58", "s59", "s60", "s61", "s62", "s63", "s64", "s65", "s66", "s67", "s68", "s69", "s70", "s71", "s72", "s73", "s74", "s75", "s76", "s77", "s78", "s79", "s80", "s81", "s82", "s83", "m0", "vcc", "scc", "memory");
    if (__builtin_expect((bad[0] | bad[1]) != 0, 0))
        o = attn_exact(__builtin_amdgcn_make_buffer_rsrc((void*)((unsigned long long)rw[1] << 32 | rw[0]), 0, (int)WS_END, 0x00020000), t2[C_head - 32], qm, qr, l16 >> 4);
    asm volatile(
  "v_rcp_f32 v12, v12\n"
  "v_lshrrev_b32 v152, 1, v156\n"
  "v_pk_mul_f32 v[0:1], v[0:1], v[12:13] op_sel_hi:[1,0]\n"
  "v_pk_mul_f32 v[2:3], v[2:3], v[12:13] op_sel_hi:[1,0]\n"
  "v_pk_mul_f32 v[4:5], v[4:5], v[12:13] op_sel_hi:[1,0]\n"
  "v_pk_mul_f32 v[6:7], v[6:7], v[12:13] op_sel_hi:[1,0]\n"
  "v_pk_mul_f32 v[8:9], v[8:9], v[12:13] op_sel_hi:[1,0]\n"
  "v_pk_mul_f32 v[10:11], v[10:11], v[12:13] op_sel_hi:[1,0]\n"
  "v_cvt_pk_bf16_f32 v0, v0, v1\n"
  "v_cvt_pk_bf16_f32 v1, v2, v3\n"
  "v_cvt_pk_bf16_f32 v2, v4, v5\n"
  "v_cvt_pk_bf16_f32 v3, v6, v7\n"
  "v_cvt_pk_bf16_f32 v4, v8, v9\n"
  "v_cvt_pk_bf16_f32 v5, v10, v11\n"
  "buffer_store_dwordx4 v[0:3], v156, s[16:19], s86 offen sc1\n"
  "s_mov_b32 exec_hi, 0\n"
  "buffer_store_dwordx2 v[4:5], v152, s[16:19], s87 offen sc1\n"
  "s_mov_b32 exec_hi, -1\n"
  "s_barrier\n"
  "v_mov_b32 v16, s46\n"
  "v_mov_b32 v17, s47\n"
  "s_cmp_ge_u32 s3, 4\n"
  "s_cbranch_scc1 2f\n"
  "v_and_b32 v18, 0x100, v152\n"
  "v_add_u32 v152, v152, v18\n"
  "v_bfe_u32 v18, v156, 8, 1\n"
  "v_bfe_u32 v19, v156, 4, 1\n"
  "v_bfe_u32 v20, v156, 5, 1\n"
  "v_lshlrev_b32 v18, 5, v18\n"
  "v_lshl_or_b32 v18, v19, 4, v18\n"
  "v_lshl_or_b32 v18, v20, 3, v18\n"
  "v_xor_b32 v18, s96, v18\n"
  "v_bfe_u32 v19, v156, 6, 2\n"
  "v_mul_u32_u24 v20, 0xc0, v19\n"
  "v_add3_u32 v18, v18, v20, s95\n"
  "v_lshrrev_b32 v20, 9, v156\n"
  "v_mul_u32_u24 v21, 0x300, v20\n"
  "v_add_u32 v153, v18, v21\n"
  "v_mad_u32_u24 v154, v21, 10, v18\n"
  "v_lshl_add_u32 v155, v21, 3, v18\n"
  "v_bfe_u32 v21, v156, 4, 5\n"
  "v_mul_u32_u24 v21, 0x500, v21\n"
  "v_lshl_add_u32 v21, v20, 4, v21\n"
  "v_add_u32 v157, s93, v21\n"
  "v_add_u32 v158, s97, v156\n"
  "v_add_u32 v159, s98, v156\n"
  "v_add_u32 v160, s99, v156\n"
  "v_bfe_u32 v20, v156, 4, 2\n"
  "v_lshl_add_u32 v20, v20, 3, s94\n"
  "v_lshl_add_u32 v161, v19, 7, v20\n"
  "2:\n"
  "s_mov_b64 exec, 1\n"
  "s_waitcnt vmcnt(0)\n"
  "buffer_store_dwordx2 v[16:17], off, s[16:19], s88 sc1\n"
  "s_mov_b64 exec, -1\n"
  "s_cmp_ge_u32 s3, 4\n"
  "s_cbranch_scc1 9f\n"
  "s_cmp_ge_u32 s3, 2\n"
  "s_cbranch_scc1 1f\n"
  "s_mov_b32 s41, 0\n"
  "buffer_load_dwordx2 v[48:49], v161, s[16:19], 0 offen sc1\n"
  "ds_read_b64_tr_b16 v[120:121], v153 offset:0\n"
  "ds_read_b64_tr_b16 v[122:123], v153 offset:1536\n"
  "ds_read_b64_tr_b16 v[124:125], v153 offset:64\n"
  "ds_read_b64_tr_b16 v[126:127], v153 offset:1600\n"
  "ds_read_b64_tr_b16 v[128:129], v153 offset:128\n"
  "ds_read_b64_tr_b16 v[130:131], v153 offset:1664\n"
  "ds_read_b64_tr_b16 v[132:133], v153 offset:3840\n"
  "ds_read_b64_tr_b16 v[134:135], v153 offset:6144\n"
  "ds_read_b64_tr_b16 v[136:137], v153 offset:3904\n"
  "ds_read_b64_tr_b16 v[138:139], v153 offset:6208\n"
  "ds_read_b64_tr_b16 v[140:141], v153 offset:3968\n"
  "ds_read_b64_tr_b16 v[142:143], v153 offset:6272\n"
  "6:\n"
  "s_waitcnt vmcnt(0)\n"
  "v_cmp_ne_u64 vcc, s[46:47], v[48:49]\n"
  "s_cbranch_vccz 7f\n"
  "buffer_load_dwordx2 v[48:49], v161, s[16:19], 0 offen sc1\n"
  "s_add_u32 s41, s41, 1\n"
  "s_cmp_le_u32 s41, 524288\n"
  "s_cbranch_scc1 6b\n"
  "7:\n"
  "s_add_u32 s40, s90, 0\n"
  "buffer_load_dwordx4 v[60:63], v156, s[16:19], s40 offen offset:0 sc1\n"
  "buffer_load_dwordx4 v[64:67], v156, s[16:19], s40 offen offset:1024 sc1\n"
  "buffer_load_dwordx4 v[68:71], v156, s[16:19], s40 offen offset:2048 sc1\n"
  "buffer_load_dwordx4 v[72:75], v156, s[16:19], s40 offen offset:3072 sc1\n"
  "s_barrier\n"
  "s_add_u32 s40, s90, 4096\n"
  "buffer_load_dwordx4 v[76:79], v156, s[16:19], s40 offen offset:0 sc1\n"
  "buffer_load_dwordx4 v[80:83], v156, s[16:19], s40 offen offset:1024 sc1\n"
  "buffer_load_dwordx4 v[84:87], v156, s[16:19], s40 offen offset:2048 sc1\n"
  "buffer_load_dwordx4 v[88:91], v156, s[16:19], s40 offen offset:3072 sc1\n"
  "s_add_u32 s40, s90, 8192\n"
  "buffer_load_dwordx4 v[92:95], v156, s[16:19], s40 offen offset:0 sc1\n"
  "buffer_load_dwordx4 v[96:99], v156, s[16:19], s40 offen offset:1024 sc1\n"
  "s_waitcnt vmcnt(9) lgkmcnt(6)\n"
  "v_mfma_f32_32x32x16_bf16 v[0:15], v[120:123], v[60:63], 0\n"
  "ds_read_b64_tr_b16 v[144:145], v153 offset:7680\n"
  "ds_read_b64_tr_b16 v[146:147], v153 offset:9216\n"
  "buffer_load_dwordx4 v[100:103], v156, s[16:19], s40 offen offset:2048 sc1\n"
  "v_mfma_f32_32x32x16_bf16 v[16:31], v[124:127], v[60:63], 0\n"
  "ds_read_b64_tr_b16 v[148:149], v153 offset:7744\n"
  "ds_read_b64_tr_b16 v[150:151], v153 offset:9280\n"
  "buffer_load_dwordx4 v[104:107], v156, s[16:19], s40 offen offset:3072 sc1\n"
  "v_mfma_f32_32x32x16_bf16 v[32:47], v[128:131], v[60:63], 0\n"
  "ds_read_b64_tr_b16 v[120:121], v153 offset:7808\n"
  "ds_read_b64_tr_b16 v[122:123], v153 offset:9344\n"
  "s_waitcnt vmcnt(10) lgkmcnt(6)\n"
  "v_mfma_f32_32x32x16_bf16 v[0:15], v[132:135], v[64:67], v[0:15]\n"
  "ds_read_b64_tr_b16 v[124:125], v153 offset:12288\n"
  "ds_read_b64_tr_b16 v[126:127], v153 offset:13824\n"
  "s_add_u32 s40, s90, 12288\n"
  "buffer_load_dwordx4 v[108:111], v156, s[16:19], s40 offen offset:0 sc1\n"
  "v_mfma_f32_32x32x16_bf16 v[16:31], v[136:139], v[64:67], v[16:31]\n"
  "ds_read_b64_tr_b16 v[128:129], v153 offset:12352\n"
  "ds_read_b64_tr_b16 v[130:131], v153 offset:13888\n"
  "buffer_load_dwordx4 v[112:115], v156, s[16:19], s40 offen offset:1024 sc1\n"
  "v_mfma_f32_32x32x16_bf16 v[32:47], v[140:143], v[64:67], v[32:47]\n"
  "ds_read_b64_tr_b16 v[132:133], v153 offset:12416\n"
  "ds_read_b64_tr_b16 v[134:135], v153 offset:13952\n"
  "s_waitcnt vmcnt(11) lgkmcnt(6)\n"
  "v_mfma_f32_32x32x16_bf16 v[0:15], v[144:147], v[68:71], v[0:15]\n"
  "s_barrier\n"
  "ds_read_b64_tr_b16 v[136:137], v153 offset:15360\n"
  "ds_read_b64_tr_b16 v[138:139], v153 offset:16896\n"
  "buffer_load_dwordx4 v[116:119], v156, s[16:19], s40 offen offset:2048 sc1\n"
  "v_mfma_f32_32x32x16_bf16 v[16:31], v[148:151], v[68:71], v[16:31]\n"
  "ds_read_b64_tr_b16 v[140:141], v153 offset:15424\n"
  "ds_read_b64_tr_b16 v[142:143], v153 offset:16960\n"
  "buffer_load_dwordx4 v[60:63], v156, s[16:19], s40 offen offset:3072 sc1\n"
  "v_mfma_f32_32x32x16_bf16 v[32:47], v[120:123], v[68:71], v[32:47]\n"
  "ds_read_b64_tr_b16 v[144:145], v153 offset:15488\n"
  "ds_read_b64_tr_b16 v[146:147], v153 offset:17024\n"
  "s_waitcnt vmcnt(12) lgkmcnt(6)\n"
  "v_mfma_f32_32x32x16_bf16 v[0:15], v[124:127], v[72:75], v[0:15]\n"
  "ds_read_b64_tr_b16 v[148:149], v153 offset:19200\n"
  "ds_read_b64_tr_b16 v[150:151], v153 offset:20736\n"
  "s_add_u32 s40, s90, 16384\n"
  "buffer_load_dwordx2 v[64:65], v152, s[16:19], s40 offen offset:0 sc1\n"
  "buffer_load_dwordx2 v[66:67], v152, s[16:19], s40 offen offset:256 sc1\n"
  "v_mfma_f32_32x32x16_bf16 v[16:31], v[128:131], v[72:75], v[16:31]\n"
  "ds_read_b64_tr_b16 v[120:121], v153 offset:19264\n"
  "ds_read_b64_tr_b16 v[122:123], v153 offset:20800\n"
  "buffer_load_dwordx2 v[68:69], v152, s[16:19], s40 offen offset:1024 sc1\n"
  "buffer_load_dwordx2 v[70:71], v152, s[16:19], s40 offen offset:1280 sc1\n"
  "v_mfma_f32_32x32x16_bf16 v[32:47], v[132:135], v[72:75], v[32:47]\n"
  "ds_read_b64_tr_b16 v[124:125], v153 offset:19328\n"
  "ds_read_b64_tr_b16 v[126:127], v153 offset:20864\n"
  "s_waitcnt vmcnt(15) lgkmcnt(6)\n"
  "v_mfma_f32_32x32x16_bf16 v[0:15], v[136:139], v[76:79], v[0:15]\n"
  "ds_read_b64_tr_b16 v[128:129], v153 offset:23040\n"
  "ds_read_b64_tr_b16 v[130:131], v153 offset:24576\n"
  "buffer_load_dwordx2 v[72:73], v152, s[16:19], s40 offen offset:2048 sc1\n"
  "buffer_load_dwordx2 v[74:75], v152, s[16:19], s40 offen offset:2304 sc1\n"
  "v_mfma_f32_32x32x16_bf16 v[16:31], v[140:143], v[76:79], v[16:31]\n"
  "ds_read_b64_tr_b16 v[132:133], v153 offset:23104\n"
  "ds_read_b64_tr_b16 v[134:135], v153 offset:24640\n"
  "v_mfma_f32_32x32x16_bf16 v[32:47], v[144:147], v[76:79], v[32:47]\n"
  "ds_read_b64_tr_b16 v[136:137], v153 offset:23168\n"
  "ds_read_b64_tr_b16 v[138:139], v153 offset:24704\n"
  "s_waitcnt vmcnt(16) lgkmcnt(6)\n"
  "v_mfma_f32_32x32x16_bf16 v[0:15], v[148:151], v[80:83], v[0:15]\n"
  "ds_read_b64_tr_b16 v[140:141], v153 offset:26880\n"
  "ds_read_b64_tr_b16 v[142:143], v153 offset:28416\n"
  "buffer_load_dwordx2 v[76:77], v152, s[16:19], s40 offen offset:3072 sc1\n"
  "buffer_load_dwordx2 v[78:79], v152, s[16:19], s40 offen offset:3328 sc1\n"
  "v_mfma_f32_32x32x16_bf16 v[16:31], v[120:123], v[80:83], v[16:31]\n"
  "ds_read_b64_tr_b16 v[144:145], v153 offset:26944\n"
  "ds_read_b64_tr_b16 v[146:147], v153 offset:28480\n"
  "v_mfma_f32_32x32x16_bf16 v[32:47], v[124:127], v[80:83], v[32:47]\n"
  "ds_read_b64_tr_b16 v[148:149], v153 offset:27008\n"
  "ds_read_b64_tr_b16 v[150:151], v153 offset:28544\n"
  "s_waitcnt vmcnt(17) lgkmcnt(6)\n"
  "v_mfma_f32_32x32x16_bf16 v[0:15], v[128:131], v[84:87], v[0:15]\n"
  "ds_read_b64_tr_b16 v[120:121], v153 offset:30720\n"
  "ds_read_b64_tr_b16 v[122:123], v153 offset:32256\n"
  "v_mfma_f32_32x32x16_bf16 v[16:31], v[132:135], v[84:87], v[16:31]\n"
  "ds_read_b64_tr_b16 v[124:125], v153 offset:30784\n"
  "ds_read_b64_tr_b16 v[126:127], v153 offset:32320\n"
  "v_mfma_f32_32x32x16_bf16 v[32:47], v[136:139], v[84:87], v[32:47]\n"
  "ds_read_b64_tr_b16 v[128:129], v153 offset:30848\n"
  "ds_read_b64_tr_b16 v[130:131], v153 offset:32384\n"
  "s_waitcnt vmcnt(16) lgkmcnt(6)\n"
  "v_mfma_f32_32x32x16_bf16 v[0:15], v[140:143], v[88:91], v[0:15]\n"
  "ds_read_b64_tr_b16 v[132:133], v153 offset:34560\n"
  "ds_read_b64_tr_b16 v[134:135], v153 offset:36864\n"
  "v_mfma_f32_32x32x16_bf16 v[16:31], v[144:147], v[88:91], v[16:31]\n"
  "ds_read_b64_tr_b16 v[136:137], v153 offset:34624\n"
  "ds_read_b64_tr_b16 v[138:139], v153 offset:36928\n"
  "v_mfma_f32_32x32x16_bf16 v[32:47], v[148:151], v[88:91], v[32:47]\n"
  "ds_read_b64_tr_b16 v[140:141], v153 offset:34688\n"
  "ds_read_b64_tr_b16 v[142:143], v153 offset:36992\n"
  "s_waitcnt vmcnt(15) lgkmcnt(6)\n"
  "v_mfma_f32_32x32x16_bf16 v[0:15], v[120:123], v[92:95], v[0:15]\n"
  "s_barrier\n"
  "ds_read_b64_tr_b16 v[144:145], v153 offset:38400\n"
  "ds_read_b64_tr_b16 v[146:147], v153 offset:39936\n"
  "v_mfma_f32_32x32x16_bf16 v[16:31], v[124:127], v[92:95], v[16:31]\n"
  "ds_read_b64_tr_b16 v[148:149], v153 offset:38464\n"
  "ds_read_b64_tr_b16 v[150:151], v153 offset:40000\n"
  "v_mfma_f32_32x32x16_bf16 v[32:47], v[128:131], v[92:95], v[32:47]\n"
  "ds_read_b64_tr_b16 v[120:121], v153 offset:38528\n"
  "ds_read_b64_tr_b16 v[122:123], v153 offset:40064\n"
  "s_waitcnt vmcnt(14) lgkmcnt(6)\n"
  "v_mfma_f32_32x32x16_bf16 v[0:15], v[132:135], v[96:99], v[0:15]\n"
  "ds_read_b64_tr_b16 v[124:125], v153 offset:43008\n"
  "ds_read_b64_tr_b16 v[126:127], v153 offset:44544\n"
  "v_mfma_f32_32x32x16_bf16 v[16:31], v[136:139], v[96:99], v[16:31]\n"
  "ds_read_b64_tr_b16 v[128:129], v153 offset:43072\n"
  "ds_read_b64_tr_b16 v[130:131], v153 offset:44608\n"
  "v_mfma_f32_32x32x16_bf16 v[32:47], v[140:143], v[96:99], v[32:47]\n"
  "ds_read_b64_tr_b16 v[132:133], v153 offset:43136\n"
  "ds_read_b64_tr_b16 v[134:135], v153 offset:44672\n"
  "s_waitcnt vmcnt(13) lgkmcnt(6)\n"
  "v_mfma_f32_32x32x16_bf16 v[0:15], v[144:147], v[100:103], v[0:15]\n"
  "ds_read_b64_tr_b16 v[136:137], v153 offset:46080\n"
  "ds_read_b64_tr_b16 v[138:139], v153 offset:47616\n"
  "buffer_load_dwordx4 v[48:51], v157, s[8:11], s91 offen\n"
  "v_mfma_f32_32x32x16_bf16 v[16:31], v[148:151], v[100:103], v[16:31]\n"
  "ds_read_b64_tr_b16 v[140:141], v153 offset:46144\n"
  "ds_read_b64_tr_b16 v[142:143], v153 offset:47680\n"
  "buffer_load_dwordx4 v[52:55], v157, s[8:11], 64 offen\n"
  "v_mfma_f32_32x32x16_bf16 v[32:47], v[120:123], v[100:103], v[32:47]\n"
  "ds_read_b64_tr_b16 v[144:145], v153 offset:46208\n"
  "ds_read_b64_tr_b16 v[146:147], v153 offset:47744\n"
  "buffer_load_dwordx4 v[56:59], v157, s[8:11], s92 offen\n"
  "s_waitcnt vmcnt(15) lgkmcnt(6)\n"
  "v_mfma_f32_32x32x16_bf16 v[0:15], v[124:127], v[104:107], v[0:15]\n"
  "ds_read_b64_tr_b16 v[148:149], v153 offset:49920\n"
  "ds_read_b64_tr_b16 v[150:151], v153 offset:51456\n"
  "v_mfma_f32_32x32x16_bf16 v[16:31], v[128:131], v[104:107], v[16:31]\n"
  "ds_read_b64_tr_b16 v[120:121], v153 offset:49984\n"
  "ds_read_b64_tr_b16 v[122:123], v153 offset:51520\n"
  "v_mfma_f32_32x32x16_bf16 v[32:47], v[132:135], v[104:107], v[32:47]\n"
  "ds_read_b64_tr_b16 v[124:125], v153 offset:50048\n"
  "ds_read_b64_tr_b16 v[126:127], v153 offset:51584\n"
  "s_waitcnt vmcnt(14) lgkmcnt(6)\n"
  "v_mfma_f32_32x32x16_bf16 v[0:15], v[136:139], v[108:111], v[0:15]\n"
  "ds_read_b64_tr_b16 v[128:129], v153 offset:53760\n"
  "ds_read_b64_tr_b16 v[130:131], v153 offset:55296\n"
  "v_mfma_f32_32x32x16_bf16 v[16:31], v[140:143], v[108:111], v[16:31]\n"
  "ds_read_b64_tr_b16 v[132:133], v153 offset:53824\n"
  "ds_read_b64_tr_b16 v[134:135], v153 offset:55360\n"
  "v_mfma_f32_32x32x16_bf16 v[32:47], v[144:147], v[108:111], v[32:47]\n"
  "ds_read_b64_tr_b16 v[136:137], v153 offset:53888\n"
  "ds_read_b64_tr_b16 v[138:139], v153 offset:55424\n"
  "s_waitcnt vmcnt(13) lgkmcnt(6)\n"
  "v_mfma_f32_32x32x16_bf16 v[0:15], v[148:151], v[112:115], v[0:15]\n"
  "ds_read_b64_tr_b16 v[140:141], v153 offset:57600\n"
  "ds_read_b64_tr_b16 v[142:143], v153 offset:59136\n"
  "v_mfma_f32_32x32x16_bf16 v[16:31], v[120:123], v[112:115], v[16:31]\n"
  "ds_read_b64_tr_b16 v[144:145], v153 offset:57664\n"
  "ds_read_b64_tr_b16 v[146:147], v153 offset:59200\n"
  "v_mfma_f32_32x32x16_bf16 v[32:47], v[124:127], v[112:115], v[32:47]\n"
  "ds_read_b64_tr_b16 v[148:149], v153 offset:57728\n"
  "ds_read_b64_tr_b16 v[150:151], v153 offset:59264\n"
  "s_waitcnt vmcnt(12) lgkmcnt(6)\n"
  "v_mfma_f32_32x32x16_bf16 v[0:15], v[128:131], v[116:119], v[0:15]\n"
  "ds_read_b64_tr_b16 v[120:121], v154 offset:3072\n"
  "ds_read_b64_tr_b16 v[122:123], v155 offset:5376\n"
  "v_mfma_f32_32x32x16_bf16 v[16:31], v[132:135], v[116:119], v[16:31]\n"
  "ds_read_b64_tr_b16 v[124:125], v154 offset:3136\n"
  "ds_read_b64_tr_b16 v[126:127], v155 offset:5440\n"
  "v_mfma_f32_32x32x16_bf16 v[32:47], v[136:139], v[116:119], v[32:47]\n"
  "ds_read_b64_tr_b16 v[128:129], v154 offset:3200\n"
  "ds_read_b64_tr_b16 v[130:131], v155 offset:5504\n"
  "s_waitcnt vmcnt(11) lgkmcnt(6)\n"
  "v_mfma_f32_32x32x16_bf16 v[0:15], v[140:143], v[60:63], v[0:15]\n"
  "ds_read_b64_tr_b16 v[132:133], v154 offset:18432\n"
  "ds_read_b64_tr_b16 v[134:135], v154 offset:22272\n"
  "v_mfma_f32_32x32x16_bf16 v[16:31], v[144:147], v[60:63], v[16:31]\n"
  "ds_read_b64_tr_b16 v[136:137], v154 offset:18496\n"
  "ds_read_b64_tr_b16 v[138:139], v154 offset:22336\n"
  "v_mfma_f32_32x32x16_bf16 v[32:47], v[148:151], v[60:63], v[32:47]\n"
  "ds_read_b64_tr_b16 v[140:141], v154 offset:18560\n"
  "ds_read_b64_tr_b16 v[142:143], v154 offset:22400\n"
  "s_waitcnt vmcnt(9) lgkmcnt(6)\n"
  "v_mfma_f32_32x32x16_bf16 v[0:15], v[120:123], v[64:67], v[0:15]\n"
  "ds_read_b64_tr_b16 v[144:145], v154 offset:33792\n"
  "ds_read_b64_tr_b16 v[146:147], v155 offset:36096\n"
  "v_mfma_f32_32x32x16_bf16 v[16:31], v[124:127], v[64:67], v[16:31]\n"
  "ds_read_b64_tr_b16 v[148:149], v154 offset:33856\n"
  "ds_read_b64_tr_b16 v[150:151], v155 offset:36160\n"
  "v_mfma_f32_32x32x16_bf16 v[32:47], v[128:131], v[64:67], v[32:47]\n"
  "ds_read_b64_tr_b16 v[120:121], v154 offset:33920\n"
  "ds_read_b64_tr_b16 v[122:123], v155 offset:36224\n"
  "s_waitcnt vmcnt(7) lgkmcnt(6)\n"
  "v_mfma_f32_32x32x16_bf16 v[0:15], v[132:135], v[68:71], v[0:15]\n"
  "ds_read_b64_tr_b16 v[124:125], v154 offset:49152\n"
  "ds_read_b64_tr_b16 v[126:127], v154 offset:52992\n"
  "v_mfma_f32_32x32x16_bf16 v[16:31], v[136:139], v[68:71], v[16:31]\n"
  "ds_read_b64_tr_b16 v[128:129], v154 offset:49216\n"
  "ds_read_b64_tr_b16 v[130:131], v154 offset:53056\n"
  "v_mfma_f32_32x32x16_bf16 v[32:47], v[140:143], v[68:71], v[32:47]\n"
  "ds_read_b64_tr_b16 v[132:133], v154 offset:49280\n"
  "ds_read_b64_tr_b16 v[134:135], v154 offset:53120\n"
  "s_waitcnt vmcnt(5) lgkmcnt(6)\n"
  "v_mfma_f32_32x32x16_bf16 v[0:15], v[144:147], v[72:75], v[0:15]\n"
  "s_waitcnt vmcnt(0)\n"
  "v_lshlrev_b32 v80, 16, v48\n"
  "v_and_b32 v81, 0xffff0000, v48\n"
  "v_lshlrev_b32 v82, 16, v49\n"
  "v_and_b32 v83, 0xffff0000, v49\n"
  "v_mfma_f32_32x32x16_bf16 v[16:31], v[148:151], v[72:75], v[16:31]\n"
  "v_lshlrev_b32 v88, 16, v52\n"
  "v_and_b32 v89, 0xffff0000, v52\n"
  "v_lshlrev_b32 v90, 16, v53\n"
  "v_and_b32 v91, 0xffff0000, v53\n"
  "v_mfma_f32_32x32x16_bf16 v[32:47], v[120:123], v[72:75], v[32:47]\n"
  "v_lshlrev_b32 v96, 16, v56\n"
  "v_and_b32 v97, 0xffff0000, v56\n"
  "v_lshlrev_b32 v98, 16, v57\n"
  "v_and_b32 v99, 0xffff0000, v57\n"
  "s_waitcnt lgkmcnt(0)\n"
  "v_mfma_f32_32x32x16_bf16 v[0:15], v[124:127], v[76:79], v[0:15]\n"
  "v_lshlrev_b32 v84, 16, v50\n"
  "v_and_b32 v85, 0xffff0000, v50\n"
  "v_lshlrev_b32 v86, 16, v51\n"
  "v_and_b32 v87, 0xffff0000, v51\n"
  "v_mfma_f32_32x32x16_bf16 v[16:31], v[128:131], v[76:79], v[16:31]\n"
  "v_lshlrev_b32 v92, 16, v54\n"
  "v_and_b32 v93, 0xffff0000, v54\n"
  "v_lshlrev_b32 v94, 16, v55\n"
  "v_and_b32 v95, 0xffff0000, v55\n"
  "v_mfma_f32_32x32x16_bf16 v[32:47], v[132:135], v[76:79], v[32:47]\n"
  "v_lshlrev_b32 v100, 16, v58\n"
  "v_and_b32 v101, 0xffff0000, v58\n"
  "v_lshlrev_b32 v102, 16, v59\n"
  "v_and_b32 v103, 0xffff0000, v59\n"
  "s_nop 3\n"
  "ds_write_b128 v158, v[8:11] offset:0\n"
  "ds_write_b128 v158, v[12:15] offset:1024\n"
  "ds_write_b128 v158, v[24:27] offset:2048\n"
  "ds_write_b128 v158, v[28:31] offset:3072\n"
  "s_nop 1\n"
  "ds_write_b128 v158, v[40:43] offset:4096\n"
  "ds_write_b128 v158, v[44:47] offset:5120\n"
  "s_waitcnt lgkmcnt(0)\n"
  "s_barrier\n"
  "ds_read_b128 v[8:11], v159 offset:0\n"
  "ds_read_b128 v[12:15], v159 offset:1024\n"
  "ds_read_b128 v[120:123], v160 offset:0\n"
  "ds_read_b128 v[124:127], v160 offset:10240\n"
  "ds_read_b128 v[24:27], v159 offset:2048\n"
  "ds_read_b128 v[28:31], v159 offset:3072\n"
  "ds_read_b128 v[128:131], v160 offset:1024\n"
  "ds_read_b128 v[132:135], v160 offset:11264\n"
  "ds_read_b128 v[40:43], v159 offset:4096\n"
  "ds_read_b128 v[44:47], v159 offset:5120\n"
  "ds_read_b128 v[136:139], v160 offset:2048\n"
  "ds_read_b128 v[140:143], v160 offset:12288\n"
  "s_waitcnt lgkmcnt(8)\n"
  "v_pk_add_f32 v[0:1], v[0:1], v[8:9]\n"
  "v_pk_add_f32 v[2:3], v[2:3], v[10:11]\n"
  "v_pk_add_f32 v[4:5], v[4:5], v[12:13]\n"
  "v_pk_add_f32 v[6:7], v[6:7], v[14:15]\n"
  "v_pk_fma_f32 v[0:1], v[0:1], v[120:121], v[80:81]\n"
  "v_pk_fma_f32 v[2:3], v[2:3], v[122:123], v[82:83]\n"
  "v_pk_fma_f32 v[4:5], v[4:5], v[124:125], v[84:85]\n"
  "v_pk_fma_f32 v[6:7], v[6:7], v[126:127], v[86:87]\n"
  "v_cvt_pk_bf16_f32 v0, v0, v1\n"
  "v_cvt_pk_bf16_f32 v1, v2, v3\n"
  "v_cvt_pk_bf16_f32 v2, v4, v5\n"
  "v_cvt_pk_bf16_f32 v3, v6, v7\n"
  "buffer_store_dwordx4 v[0:3], v157, s[32:35], s91 offen\n"
  "s_waitcnt lgkmcnt(4)\n"
  "v_pk_add_f32 v[16:17], v[16:17], v[24:25]\n"
  "v_pk_add_f32 v[18:19], v[18:19], v[26:27]\n"
  "v_pk_add_f32 v[20:21], v[20:21], v[28:29]\n"
  "v_pk_add_f32 v[22:23], v[22:23], v[30:31]\n"
  "v_pk_fma_f32 v[16:17], v[16:17], v[128:129], v[88:89]\n"
  "v_pk_fma_f32 v[18:19], v[18:19], v[130:131], v[90:91]\n"
  "v_pk_fma_f32 v[20:21], v[20:21], v[132:133], v[92:93]\n"
  "v_pk_fma_f32 v[22:23], v[22:23], v[134:135], v[94:95]\n"
  "v_cvt_pk_bf16_f32 v16, v16, v17\n"
  "v_cvt_pk_bf16_f32 v17, v18, v19\n"
  "v_cvt_pk_bf16_f32 v18, v20, v21\n"
  "v_cvt_pk_bf16_f32 v19, v22, v23\n"
  "buffer_store_dwordx4 v[16:19], v157, s[32:35], 64 offen\n"
  "s_waitcnt lgkmcnt(0)\n"
  "v_pk_add_f32 v[32:33], v[32:33], v[40:41]\n"
  "v_pk_add_f32 v[34:35], v[34:35], v[42:43]\n"
  "v_pk_add_f32 v[36:37], v[36:37], v[44:45]\n"
  "v_pk_add_f32 v[38:39], v[38:39], v[46:47]\n"
  "v_pk_fma_f32 v[32:33], v[32:33], v[136:137], v[96:97]\n"
  "v_pk_fma_f32 v[34:35], v[34:35], v[138:139], v[98:99]\n"
  "v_pk_fma_f32 v[36:37], v[36:37], v[140:141], v[100:101]\n"
  "v_pk_fma_f32 v[38:39], v[38:39], v[142:143], v[102:103]\n"
  "v_cvt_pk_bf16_f32 v32, v32, v33\n"
  "v_cvt_pk_bf16_f32 v33, v34, v35\n"
  "v_cvt_pk_bf16_f32 v34, v36, v37\n"
  "v_cvt_pk_bf16_f32 v35, v38, v39\n"
  "buffer_store_dwordx4 v[32:35], v157, s[32:35], s92 offen\n"
  "s_branch 9f\n"
  "1:\n"
  "s_mov_b32 s41, 0\n"
  "buffer_load_dwordx2 v[48:49], v161, s[16:19], 0 offen sc1\n"
  "ds_read_b64_tr_b16 v[120:121], v153 offset:0\n"
  "ds_read_b64_tr_b16 v[122:123], v153 offset:1536\n"
  "ds_read_b64_tr_b16 v[124:125], v153 offset:64\n"
  "ds_read_b64_tr_b16 v[126:127], v153 offset:1600\n"
  "ds_read_b64_tr_b16 v[128:129], v153 offset:3840\n"
  "ds_read_b64_tr_b16 v[130:131], v153 offset:6144\n"
  "ds_read_b64_tr_b16 v[132:133], v153 offset:3904\n"
  "ds_read_b64_tr_b16 v[134:135], v153 offset:6208\n"
  "ds_read_b64_tr_b16 v[136:137], v153 offset:7680\n"
  "ds_read_b64_tr_b16 v[138:139], v153 offset:9216\n"
  "ds_read_b64_tr_b16 v[140:141], v153 offset:7744\n"
  "ds_read_b64_tr_b16 v[142:143], v153 offset:9280\n"
  "6:\n"
  "s_waitcnt vmcnt(0)\n"
  "v_cmp_ne_u64 vcc, s[46:47], v[48:49]\n"
  "s_cbranch_vccz 7f\n"
  "buffer_load_dwordx2 v[48:49], v161, s[16:19], 0 offen sc1\n"
  "s_add_u32 s41, s41, 1\n"
  "s_cmp_le_u32 s41, 524288\n"
  "s_cbranch_scc1 6b\n"
  "7:\n"
  "s_add_u32 s40, s90, 0\n"
  "buffer_load_dwordx4 v[60:63], v156, s[16:19], s40 offen offset:0 sc1\n"
  "buffer_load_dwordx4 v[64:67], v156, s[16:19], s40 offen offset:1024 sc1\n"
  "buffer_load_dwordx4 v[68:71], v156, s[16:19], s40 offen offset:2048 sc1\n"
  "buffer_load_dwordx4 v[72:75], v156, s[16:19], s40 offen offset:3072 sc1\n"
  "s_barrier\n"
  "s_add_u32 s40, s90, 4096\n"
  "buffer_load_dwordx4 v[76:79], v156, s[16:19], s40 offen offset:0 sc1\n"
  "buffer_load_dwordx4 v[80:83], v156, s[16:19], s40 offen offset:1024 sc1\n"
  "buffer_load_dwordx4 v[84:87], v156, s[16:19], s40 offen offset:2048 sc1\n"
  "buffer_load_dwordx4 v[88:91], v156, s[16:19], s40 offen offset:3072 sc1\n"
  "s_add_u32 s40, s90, 8192\n"
  "buffer_load_dwordx4 v[92:95], v156, s[16:19], s40 offen offset:0 sc1\n"
  "buffer_load_dwordx4 v[96:99], v156, s[16:19], s40 offen offset:1024 sc1\n"
  "s_waitcnt vmcnt(9) lgkmcnt(8)\n"
  "v_mfma_f32_32x32x16_bf16 v[0:15], v[120:123], v[60:63], 0\n"
  "ds_read_b64_tr_b16 v[144:145], v153 offset:12288\n"
  "ds_read_b64_tr_b16 v[146:147], v153 offset:13824\n"
  "buffer_load_dwordx4 v[100:103], v156, s[16:19], s40 offen offset:2048 sc1\n"
  "v_mfma_f32_32x32x16_bf16 v[16:31], v[124:127], v[60:63], 0\n"
  "ds_read_b64_tr_b16 v[148:149], v153 offset:12352\n"
  "ds_read_b64_tr_b16 v[150:151], v153 offset:13888\n"
  "buffer_load_dwordx4 v[104:107], v156, s[16:19], s40 offen offset:3072 sc1\n"
  "s_waitcnt vmcnt(10) lgkmcnt(8)\n"
  "v_mfma_f32_32x32x16_bf16 v[0:15], v[128:131], v[64:67], v[0:15]\n"
  "ds_read_b64_tr_b16 v[120:121], v153 offset:15360\n"
  "ds_read_b64_tr_b16 v[122:123], v153 offset:16896\n"
  "s_add_u32 s40, s90, 12288\n"
  "buffer_load_dwordx4 v[108:111], v156, s[16:19], s40 offen offset:0 sc1\n"
  "v_mfma_f32_32x32x16_bf16 v[16:31], v[132:135], v[64:67], v[16:31]\n"
  "ds_read_b64_tr_b16 v[124:125], v153 offset:15424\n"
  "ds_read_b64_tr_b16 v[126:127], v153 offset:16960\n"
  "buffer_load_dwordx4 v[112:115], v156, s[16:19], s40 offen offset:1024 sc1\n"
  "s_waitcnt vmcnt(11) lgkmcnt(8)\n"
  "v_mfma_f32_32x32x16_bf16 v[0:15], v[136:139], v[68:71], v[0:15]\n"
  "ds_read_b64_tr_b16 v[128:129], v153 offset:19200\n"
  "ds_read_b64_tr_b16 v[130:131], v153 offset:20736\n"
  "buffer_load_dwordx4 v[116:119], v156, s[16:19], s40 offen offset:2048 sc1\n"
  "v_mfma_f32_32x32x16_bf16 v[16:31], v[140:143], v[68:71], v[16:31]\n"
  "ds_read_b64_tr_b16 v[132:133], v153 offset:19264\n"
  "ds_read_b64_tr_b16 v[134:135], v153 offset:20800\n"
  "buffer_load_dwordx4 v[60:63], v156, s[16:19], s40 offen offset:3072 sc1\n"
  "s_waitcnt vmcnt(12) lgkmcnt(8)\n"
  "v_mfma_f32_32x32x16_bf16 v[0:15], v[144:147], v[72:75], v[0:15]\n"
  "s_barrier\n"
  "ds_read_b64_tr_b16 v[136:137], v153 offset:23040\n"
  "ds_read_b64_tr_b16 v[138:139], v153 offset:24576\n"
  "s_add_u32 s40, s90, 16384\n"
  "buffer_load_dwordx2 v[64:65], v152, s[16:19], s40 offen offset:0 sc1\n"
  "buffer_load_dwordx2 v[66:67], v152, s[16:19], s40 offen offset:256 sc1\n"
  "v_mfma_f32_32x32x16_bf16 v[16:31], v[148:151], v[72:75], v[16:31]\n"
  "ds_read_b64_tr_b16 v[140:141], v153 offset:23104\n"
  "ds_read_b64_tr_b16 v[142:143], v153 offset:24640\n"
  "buffer_load_dwordx2 v[68:69], v152, s[16:19], s40 offen offset:1024 sc1\n"
  "buffer_load_dwordx2 v[70:71], v152, s[16:19], s40 offen offset:1280 sc1\n"
  "s_waitcnt vmcnt(15) lgkmcnt(8)\n"
  "v_mfma_f32_32x32x16_bf16 v[0:15], v[120:123], v[76:79], v[0:15]\n"
  "ds_read_b64_tr_b16 v[144:145], v153 offset:26880\n"
  "ds_read_b64_tr_b16 v[146:147], v153 offset:28416\n"
  "buffer_load_dwordx2 v[72:73], v152, s[16:19], s40 offen offset:2048 sc1\n"
  "buffer_load_dwordx2 v[74:75], v152, s[16:19], s40 offen offset:2304 sc1\n"
  "v_mfma_f32_32x32x16_bf16 v[16:31], v[124:127], v[76:79], v[16:31]\n"
  "ds_read_b64_tr_b16 v[148:149], v153 offset:26944\n"
  "ds_read_b64_tr_b16 v[150:151], v153 offset:28480\n"
  "s_waitcnt vmcnt(16) lgkmcnt(8)\n"
  "v_mfma_f32_32x32x16_bf16 v[0:15], v[128:131], v[80:83], v[0:15]\n"
  "ds_read_b64_tr_b16 v[120:121], v153 offset:30720\n"
  "ds_read_b64_tr_b16 v[122:123], v153 offset:32256\n"
  "buffer_load_dwordx2 v[76:77], v152, s[16:19], s40 offen offset:3072 sc1\n"
  "buffer_load_dwordx2 v[78:79], v152, s[16:19], s40 offen offset:3328 sc1\n"
  "v_mfma_f32_32x32x16_bf16 v[16:31], v[132:135], v[80:83], v[16:31]\n"
  "ds_read_b64_tr_b16 v[124:125], v153 offset:30784\n"
  "ds_read_b64_tr_b16 v[126:127], v153 offset:32320\n"
  "s_waitcnt vmcnt(17) lgkmcnt(8)\n"
  "v_mfma_f32_32x32x16_bf16 v[0:15], v[136:139], v[84:87], v[0:15]\n"
  "ds_read_b64_tr_b16 v[128:129], v153 offset:34560\n"
  "ds_read_b64_tr_b16 v[130:131], v153 offset:36864\n"
  "v_mfma_f32_32x32x16_bf16 v[16:31], v[140:143], v[84:87], v[16:31]\n"
  "ds_read_b64_tr_b16 v[132:133], v153 offset:34624\n"
  "ds_read_b64_tr_b16 v[134:135], v153 offset:36928\n"
  "s_waitcnt vmcnt(16) lgkmcnt(8)\n"
  "v_mfma_f32_32x32x16_bf16 v[0:15], v[144:147], v[88:91], v[0:15]\n"
  "ds_read_b64_tr_b16 v[136:137], v153 offset:38400\n"
  "ds_read_b64_tr_b16 v[138:139], v153 offset:39936\n"
  "v_mfma_f32_32x32x16_bf16 v[16:31], v[148:151], v[88:91], v[16:31]\n"
  "ds_read_b64_tr_b16 v[140:141], v153 offset:38464\n"
  "ds_read_b64_tr_b16 v[142:143], v153 offset:40000\n"
  "s_waitcnt vmcnt(15) lgkmcnt(8)\n"
  "v_mfma_f32_32x32x16_bf16 v[0:15], v[120:123], v[92:95], v[0:15]\n"
  "ds_read_b64_tr_b16 v[144:145], v153 offset:43008\n"
  "ds_read_b64_tr_b16 v[146:147], v153 offset:44544\n"
  "v_mfma_f32_32x32x16_bf16 v[16:31], v[124:127], v[92:95], v[16:31]\n"
  "ds_read_b64_tr_b16 v[148:149], v153 offset:43072\n"
  "ds_read_b64_tr_b16 v[150:151], v153 offset:44608\n"
  "s_waitcnt vmcnt(14) lgkmcnt(8)\n"
  "v_mfma_f32_32x32x16_bf16 v[0:15], v[128:131], v[96:99], v[0:15]\n"
  "ds_read_b64_tr_b16 v[120:121], v153 offset:46080\n"
  "ds_read_b64_tr_b16 v[122:123], v153 offset:47616\n"
  "v_mfma_f32_32x32x16_bf16 v[16:31], v[132:135], v[96:99], v[16:31]\n"
  "ds_read_b64_tr_b16 v[124:125], v153 offset:46144\n"
  "ds_read_b64_tr_b16 v[126:127], v153 offset:47680\n"
  "s_waitcnt vmcnt(13) lgkmcnt(8)\n"
  "v_mfma_f32_32x32x16_bf16 v[0:15], v[136:139], v[100:103], v[0:15]\n"
  "ds_read_b64_tr_b16 v[128:129], v153 offset:49920\n"
  "ds_read_b64_tr_b16 v[130:131], v153 offset:51456\n"
  "buffer_load_dwordx4 v[48:51], v157, s[8:11], s91 offen\n"
  "v_mfma_f32_32x32x16_bf16 v[16:31], v[140:143], v[100:103], v[16:31]\n"
  "ds_read_b64_tr_b16 v[132:133], v153 offset:49984\n"
  "ds_read_b64_tr_b16 v[134:135], v153 offset:51520\n"
  "buffer_load_dwordx4 v[52:55], v157, s[8:11], 64 offen\n"
  "s_waitcnt vmcnt(14) lgkmcnt(8)\n"
  "v_mfma_f32_32x32x16_bf16 v[0:15], v[144:147], v[104:107], v[0:15]\n"
  "ds_read_b64_tr_b16 v[136:137], v153 offset:53760\n"
  "ds_read_b64_tr_b16 v[138:139], v153 offset:55296\n"
  "v_mfma_f32_32x32x16_bf16 v[16:31], v[148:151], v[104:107], v[16:31]\n"
  "ds_read_b64_tr_b16 v[140:141], v153 offset:53824\n"
  "ds_read_b64_tr_b16 v[142:143], v153 offset:55360\n"
  "s_waitcnt vmcnt(13) lgkmcnt(8)\n"
  "v_mfma_f32_32x32x16_bf16 v[0:15], v[120:123], v[108:111], v[0:15]\n"
  "s_barrier\n"
  "ds_read_b64_tr_b16 v[144:145], v153 offset:57600\n"
  "ds_read_b64_tr_b16 v[146:147], v153 offset:59136\n"
  "v_mfma_f32_32x32x16_bf16 v[16:31], v[124:127], v[108:111], v[16:31]\n"
  "ds_read_b64_tr_b16 v[148:149], v153 offset:57664\n"
  "ds_read_b64_tr_b16 v[150:151], v153 offset:59200\n"
  "s_waitcnt vmcnt(12) lgkmcnt(8)\n"
  "v_mfma_f32_32x32x16_bf16 v[0:15], v[128:131], v[112:115], v[0:15]\n"
  "ds_read_b64_tr_b16 v[120:121], v154 offset:3072\n"
  "ds_read_b64_tr_b16 v[122:123], v155 offset:5376\n"
  "v_mfma_f32_32x32x16_bf16 v[16:31], v[132:135], v[112:115], v[16:31]\n"
  "ds_read_b64_tr_b16 v[124:125], v154 offset:3136\n"
  "ds_read_b64_tr_b16 v[126:127], v155 offset:5440\n"
  "s_waitcnt vmcnt(11) lgkmcnt(8)\n"
  "v_mfma_f32_32x32x16_bf16 v[0:15], v[136:139], v[116:119], v[0:15]\n"
  "ds_read_b64_tr_b16 v[128:129], v154 offset:18432\n"
  "ds_read_b64_tr_b16 v[130:131], v154 offset:22272\n"
  "v_mfma_f32_32x32x16_bf16 v[16:31], v[140:143], v[116:119], v[16:31]\n"
  "ds_read_b64_tr_b16 v[132:133], v154 offset:18496\n"
  "ds_read_b64_tr_b16 v[134:135], v154 offset:22336\n"
  "s_waitcnt vmcnt(10) lgkmcnt(8)\n"
  "v_mfma_f32_32x32x16_bf16 v[0:15], v[144:147], v[60:63], v[0:15]\n"
  "ds_read_b64_tr_b16 v[136:137], v154 offset:33792\n"
  "ds_read_b64_tr_b16 v[138:139], v155 offset:36096\n"
  "v_mfma_f32_32x32x16_bf16 v[16:31], v[148:151], v[60:63], v[16:31]\n"
  "ds_read_b64_tr_b16 v[140:141], v154 offset:33856\n"
  "ds_read_b64_tr_b16 v[142:143], v155 offset:36160\n"
  "s_waitcnt vmcnt(8) lgkmcnt(8)\n"
  "v_mfma_f32_32x32x16_bf16 v[0:15], v[120:123], v[64:67], v[0:15]\n"
  "ds_read_b64_tr_b16 v[144:145], v154 offset:49152\n"
  "ds_read_b64_tr_b16 v[146:147], v154 offset:52992\n"
  "v_mfma_f32_32x32x16_bf16 v[16:31], v[124:127], v[64:67], v[16:31]\n"
  "ds_read_b64_tr_b16 v[148:149], v154 offset:49216\n"
  "ds_read_b64_tr_b16 v[150:151], v154 offset:53056\n"
  "s_waitcnt vmcnt(6) lgkmcnt(8)\n"
  "v_mfma_f32_32x32x16_bf16 v[0:15], v[128:131], v[68:71], v[0:15]\n"
  "v_mfma_f32_32x32x16_bf16 v[16:31], v[132:135], v[68:71], v[16:31]\n"
  "s_waitcnt vmcnt(4) lgkmcnt(4)\n"
  "v_mfma_f32_32x32x16_bf16 v[0:15], v[136:139], v[72:75], v[0:15]\n"
  "s_waitcnt vmcnt(0)\n"
  "v_lshlrev_b32 v80, 16, v48\n"
  "v_and_b32 v81, 0xffff0000, v48\n"
  "v_lshlrev_b32 v82, 16, v49\n"
  "v_and_b32 v83, 0xffff0000, v49\n"
  "v_mfma_f32_32x32x16_bf16 v[16:31], v[140:143], v[72:75], v[16:31]\n"
  "v_lshlrev_b32 v88, 16, v52\n"
  "v_and_b32 v89, 0xffff0000, v52\n"
  "v_lshlrev_b32 v90, 16, v53\n"
  "v_and_b32 v91, 0xffff0000, v53\n"
  "s_waitcnt lgkmcnt(0)\n"
  "v_mfma_f32_32x32x16_bf16 v[0:15], v[144:147], v[76:79], v[0:15]\n"
  "v_lshlrev_b32 v84, 16, v50\n"
  "v_and_b32 v85, 0xffff0000, v50\n"
  "v_lshlrev_b32 v86, 16, v51\n"
  "v_and_b32 v87, 0xffff0000, v51\n"
  "v_mfma_f32_32x32x16_bf16 v[16:31], v[148:151], v[76:79], v[16:31]\n"
  "v_lshlrev_b32 v92, 16, v54\n"
  "v_and_b32 v93, 0xffff0000, v54\n"
  "v_lshlrev_b32 v94, 16, v55\n"
  "v_and_b32 v95, 0xffff0000, v55\n"
  "s_nop 3\n"
  "ds_write_b128 v158, v[8:11] offset:0\n"
  "ds_write_b128 v158, v[12:15] offset:1024\n"
  "s_nop 1\n"
  "ds_write_b128 v158, v[24:27] offset:2048\n"
  "ds_write_b128 v158, v[28:31] offset:3072\n"
  "s_waitcnt lgkmcnt(0)\n"
  "s_barrier\n"
  "ds_read_b128 v[8:11], v159 offset:0\n"
  "ds_read_b128 v[12:15], v159 offset:1024\n"
  "ds_read_b128 v[120:123], v160 offset:0\n"
  "ds_read_b128 v[124:127], v160 offset:10240\n"
  "ds_read_b128 v[24:27], v159 offset:2048\n"
  "ds_read_b128 v[28:31], v159 offset:3072\n"
  "ds_read_b128 v[128:131], v160 offset:1024\n"
  "ds_read_b128 v[132:135], v160 offset:11264\n"
  "s_waitcnt lgkmcnt(4)\n"
  "v_pk_add_f32 v[0:1], v[0:1], v[8:9]\n"
  "v_pk_add_f32 v[2:3], v[2:3], v[10:11]\n"
  "v_pk_add_f32 v[4:5], v[4:5], v[12:13]\n"
  "v_pk_add_f32 v[6:7], v[6:7], v[14:15]\n"
  "v_pk_fma_f32 v[0:1], v[0:1], v[120:121], v[80:81]\n"
  "v_pk_fma_f32 v[2:3], v[2:3], v[122:123], v[82:83]\n"
  "v_pk_fma_f32 v[4:5], v[4:5], v[124:125], v[84:85]\n"
  "v_pk_fma_f32 v[6:7], v[6:7], v[126:127], v[86:87]\n"
  "v_cvt_pk_bf16_f32 v0, v0, v1\n"
  "v_cvt_pk_bf16_f32 v1, v2, v3\n"
  "v_cvt_pk_bf16_f32 v2, v4, v5\n"
  "v_cvt_pk_bf16_f32 v3, v6, v7\n"
  "buffer_store_dwordx4 v[0:3], v157, s[32:35], s91 offen\n"
  "s_waitcnt lgkmcnt(0)\n"
  "v_pk_add_f32 v[16:17], v[16:17], v[24:25]\n"
  "v_pk_add_f32 v[18:19], v[18:19], v[26:27]\n"
  "v_pk_add_f32 v[20:21], v[20:21], v[28:29]\n"
  "v_pk_add_f32 v[22:23], v[22:23], v[30:31]\n"
  "v_pk_fma_f32 v[16:17], v[16:17], v[128:129], v[88:89]\n"
  "v_pk_fma_f32 v[18:19], v[18:19], v[130:131], v[90:91]\n"
  "v_pk_fma_f32 v[20:21], v[20:21], v[132:133], v[92:93]\n"
  "v_pk_fma_f32 v[22:23], v[22:23], v[134:135], v[94:95]\n"
  "v_cvt_pk_bf16_f32 v16, v16, v17\n"
  "v_cvt_pk_bf16_f32 v17, v18, v19\n"
  "v_cvt_pk_bf16_f32 v18, v20, v21\n"
  "v_cvt_pk_bf16_f32 v19, v22, v23\n"
  "buffer_store_dwordx4 v[16:19], v157, s[32:35], 64 offen\n"
  "9:\n"
        : "+{v[0:15]}"(o), "+{v167}"(dbg)
        : "{v156}"(l16), "{s2}"(blockIdx.x), "{s3}"(wid), "{s[8:11]}"(rx), "{s[16:19]}"(rw), "{s[32:35]}"(ro), "{s[46:47]}"(tag), "{s[84:99]}"(t2)
        : "v16", "v17", "v18", "v19", "v20", "v21", "v22", "v23", "v24", "v25", "v26", "v27", "v28", "v29", "v30", "v31", "v32", "v33", "v34", "v35", "v36", "v37", "v38", "v39", "v40", "v41", "v42", "v43", "v44", "v45", "v46", "v47", "v48", "v49", "v50", "v51", "v52", "v53", "v54", "v55", "v56", "v57", "v58", "v59", "v60", "v61", "v62", "v63", "v64", "v65", "v66", "v67", "v68", "v69", "v70", "v71", "v72", "v73", "v74", "v75", "v76", "v77", "v78", "v79", "v80", "v81", "v82", "v83", "v84", "v85", "v86", "v87", "v88", "v89", "v90", "v91", "v92", "v93", "v94", "v95", "v96", "v97", "v98", "v99", "v100", "v101", "v102", "v103", "v104", "v105", "v106", "v107", "v108", "v109", "v110", "v111", "v112", "v113", "v114", "v115", "v116", "v117", "v118", "v119", "v120", "v121", "v122", "v123", "v124", "v125", "v126", "v127", "v128", "v129", "v130", "v131", "v132", "v133", "v134", "v135", "v136", "v137", "v138", "v139", "v140", "v141", "v142", "v143", "v144", "v145", "v146", "v147", "v148", "v149", "v150", "v151", "v152", "v153", "v154", "v155", "v157", "v158", "v159", "v160", "v161", "v162", "v163", "v164", "v165", "v166", "s40", "s41", "s42", "s43", "s44", "s45", "vcc", "scc", "memory");
}
}

extern "C" void kernel_launch(void* const* d_in, const int* in_sizes, int n_in, void* d_out, int out_size, void* d_ws, size_t ws_size, hipStream_t stream) {
    const unsigned F = 0x00020000;
    fused_block<<<256, 768, 0, stream>>>(d_in[0], 2048 * 1280, F, d_in[1], 640 * 3840, F, d_ws, WS_END, F, d_in[5], 1536 * 1280, F, d_in[4], 640 * 3072, F, d_in[2], 640 * 1280, F, d_out, 2048 * 1280, F);
}
```
